```python
import torch

def _softmax(x, axis=-1):
    return torch.softmax(x, dim=axis)
def _gelu(x):
    return torch.nn.functional.gelu(x, approximate="tanh")
def _silu(x):
    return torch.nn.functional.silu(x)
def _sigmoid(x):
    return torch.sigmoid(x)
def _layernorm(x, axis=-1, eps=1e-5):
    m = x.mean(dim=axis, keepdim=True)
    v = x.var(dim=axis, keepdim=True, unbiased=False)
    return (x - m) / torch.sqrt(v + eps)
def _scatter_add(tgt, idx, src, axis=0):
    return tgt.clone().index_add_(axis, idx, src)
def _segment_sum(src, seg, n):
    out = torch.zeros((n,) + src.shape[1:], dtype=src.dtype)
    return out.index_add_(0, seg, src)
def _pad(x, pw):
    flat = tuple(v for pair in reversed(pw) for v in pair)
    return torch.nn.functional.pad(x, flat)

kernel_name = 'gated_mix__spectral_fnet__sliding_local_dilated__prompt'

def setup_inputs():

    _w = (lambda a: (lambda t: t.to(torch.bfloat16) if t.is_floating_point() else t)(torch.as_tensor(a)))
    def _A_setup():
        B, S, D = 1, 2048, 640
        ns = torch.arange(S)[:, None] * torch.arange(S)[None, :]
        nd = torch.arange(D)[:, None] * torch.arange(D)[None, :]
        Ws_re = torch.cos(-2.0 * torch.pi * ns / S).to(float)
        Ws_im = torch.sin(-2.0 * torch.pi * ns / S).to(float)
        Wd_re = torch.cos(-2.0 * torch.pi * nd / D).to(float)
        Wd_im = torch.sin(-2.0 * torch.pi * nd / D).to(float)
        return {
            "x": _w((lambda *s: torch.randn(*s, dtype=torch.bfloat16) * 0.05)(B, S, D)),
            "dft_seq_re": _w(Ws_re),
            "dft_seq_im": _w(Ws_im),
            "dft_hid_re": _w(Wd_re),
            "dft_hid_im": _w(Wd_im),
        }
    _A_in = _A_setup()

    def _B_setup():
        B, S, D = 1, 2048, 640
        return {
            "x": _w((lambda *s: torch.randn(*s, dtype=torch.bfloat16) * 0.05)(B, S, D)),
            "wqkv": _w((lambda *s: torch.randn(*s, dtype=torch.bfloat16) * 1.6)(D, 3 * D)),
            "wo": _w((lambda *s: torch.randn(*s, dtype=torch.bfloat16) * 0.2)(D, D)),
        }
    _B_in = _B_setup()

    return {
        "A_x": _A_in["x"],
        "A_dft_seq_re": _A_in["dft_seq_re"],
        "A_dft_seq_im": _A_in["dft_seq_im"],
        "A_dft_hid_re": _A_in["dft_hid_re"],
        "A_dft_hid_im": _A_in["dft_hid_im"],
        "B_x": _B_in["x"],
        "B_wqkv": _B_in["wqkv"],
        "B_wo": _B_in["wo"],
        "gate_w": _w((lambda *s: torch.randn(*s, dtype=torch.bfloat16) * 0.05)(640) * 16.0),
    }

def reference(A_x, A_dft_seq_re, A_dft_seq_im, A_dft_hid_re, A_dft_hid_im, B_x, B_wqkv, B_wo, gate_w):
    def _A_ref(x, dft_seq_re, dft_seq_im, dft_hid_re, dft_hid_im):
        a = torch.einsum("bsd,ed->bse", x, dft_hid_re)
        b = torch.einsum("bsd,ed->bse", x, dft_hid_im)
        y = torch.einsum("ts,bse->bte", dft_seq_re, a) - torch.einsum("ts,bse->bte", dft_seq_im, b)
        return y

    def _B_ref(x, wqkv, wo):
        B, S, D = x.shape
        H = 32; Dh = D // H
        W, dil = 8, 2
        qkv = (lambda x, *d: x.permute(*d))((x @ wqkv).reshape(B, S, 3, H, Dh), 2, 0, 3, 1, 4)
        q, k, v = qkv[0], qkv[1], qkv[2]
        scale = Dh ** -0.5
        pos = torch.arange(S)
        off = torch.arange(W) * dil
        idx = pos[:, None] - off[None, :]
        mask = idx >= 0
        idx_c = torch.where(mask, idx, 0).reshape(-1)
        kg = k[:, :, idx_c, :].reshape(B, H, S, W, Dh)
        vg = v[:, :, idx_c, :].reshape(B, H, S, W, Dh)
        logits = torch.einsum("bhsd,bhswd->bhsw", q, kg) * scale
        logits = torch.where(mask[None, None, :, :], logits, -1e9)
        attn = _softmax(logits, axis=-1)
        o = torch.einsum("bhsw,bhswd->bhsd", attn, vg)
        return (lambda x, *d: x.permute(*d))(o, 0, 2, 1, 3).reshape(B, S, D) @ wo

    x = A_x
    g = _sigmoid((x * gate_w).sum(-1, keepdims=True))
    a = _A_ref(x, A_dft_seq_re, A_dft_seq_im, A_dft_hid_re, A_dft_hid_im)
    b = _B_ref(x, B_wqkv, B_wo)
    return g * a + (1 - g) * b
```

```cpp
#include <hip/hip_runtime.h>
#include <hip/hip_bf16.h>
typedef unsigned short u16;
__device__ __forceinline__ float bf2f(u16 v){ return __uint_as_float(((unsigned)v)<<16); }
__device__ __forceinline__ u16 f2bf(float f){ __hip_bfloat16 h = __float2bfloat16(f); return *(u16*)&h; }
constexpr int S=2048, D=640, H=32, DH=20;

__global__ void gate_k(const u16* x, const u16* gw, float* g){
  int s = blockIdx.x; float acc=0;
  for(int d=threadIdx.x; d<D; d+=64){ acc += bf2f(x[s*D+d])*bf2f(gw[d]); }
  for(int o=32;o>0;o>>=1) acc += __shfl_xor(acc,o);
  if(threadIdx.x==0) g[s] = 1.f/(1.f+__expf(-acc));
}
template<int MODE>
__global__ void gemm_k(const u16* A, int lda, const u16* B, int sk, int sn, int K, u16* C, int ldc, const float* g){
  __shared__ float As[16][17], Bs[16][17];
  int tx=threadIdx.x&15, ty=threadIdx.x>>4;
  int m=blockIdx.y*16+ty, n=blockIdx.x*16+tx; float acc=0;
  for(int k0=0;k0<K;k0+=16){
    As[ty][tx]=bf2f(A[(long)m*lda+k0+tx]);
    Bs[ty][tx]=bf2f(B[(long)(k0+ty)*sk+(long)n*sn]);
    __syncthreads();
    for(int k=0;k<16;k++) acc+=As[ty][k]*Bs[k][tx];
    __syncthreads();
  }
  if(MODE==1) acc*=g[m];
  if(MODE==2) acc*=-g[m];
  C[(long)m*ldc+n]=f2bf(acc);
}
__global__ void attn_k(const u16* qkv, const float* g, u16* A2){
  int s=blockIdx.x, h=threadIdx.x; if(h>=H) return;
  float q[DH]; for(int d=0;d<DH;d++) q[d]=bf2f(qkv[s*1920+h*DH+d]);
  float l[8]; float mx=-1e30f;
  for(int w=0;w<8;w++){ int t=s-2*w; float a=0; if(t>=0){ for(int d=0;d<DH;d++) a+=q[d]*bf2f(qkv[t*1920+640+h*DH+d]); a*=0.22360679774997896f;} else a=-1e9f; l[w]=a; mx=fmaxf(mx,a);}
  float sum=0; for(int w=0;w<8;w++){ l[w]=__expf(l[w]-mx); sum+=l[w]; }
  float sc=(1.f-g[s])/sum;
  for(int d=0;d<DH;d++){ float o=0; for(int w=0;w<8;w++){ int t=s-2*w; if(t<0)t=0; o+=l[w]*bf2f(qkv[t*1920+1280+h*DH+d]); } A2[s*1920+1280+h*DH+d]=f2bf(o*sc); }
}
__global__ void final_k(const u16* A2, const u16* Wr, const u16* Wi, const u16* wo, u16* out){
  __shared__ float As[16][17], Bs[16][17];
  int tx=threadIdx.x&15, ty=threadIdx.x>>4;
  int m=blockIdx.y*16+ty, n=blockIdx.x*16+tx; float acc=0;
  for(int k0=0;k0<1920;k0+=16){
    As[ty][tx]=bf2f(A2[(long)m*1920+k0+tx]);
    int k=k0+ty; float b;
    if(k<640) b=bf2f(Wr[n*640+k]); else if(k<1280) b=bf2f(Wi[n*640+k-640]); else b=bf2f(wo[(k-1280)*640+n]);
    Bs[ty][tx]=b;
    __syncthreads();
    for(int k=0;k<16;k++) acc+=As[ty][k]*Bs[k][tx];
    __syncthreads();
  }
  out[(long)m*640+n]=f2bf(acc);
}
extern "C" void kernel_launch(void* const* d_in, const int* in_sizes, int n_in, void* d_out, int out_size, void* d_ws, size_t ws_size, hipStream_t stream){
  const u16* x=(const u16*)d_in[0]; const u16* Cr=(const u16*)d_in[1]; const u16* Ci=(const u16*)d_in[2];
  const u16* Wr=(const u16*)d_in[3]; const u16* Wi=(const u16*)d_in[4]; const u16* wqkv=(const u16*)d_in[6];
  const u16* wo=(const u16*)d_in[7]; const u16* gw=(const u16*)d_in[8];
  char* ws=(char*)d_ws; float* g=(float*)ws; u16* A2=(u16*)(ws+65536); u16* qkv=A2+2048*1920;
  gate_k<<<S,64,0,stream>>>(x,gw,g);
  gemm_k<1><<<dim3(40,128),256,0,stream>>>(Cr,2048,x,640,1,2048,A2,1920,g);
  gemm_k<2><<<dim3(40,128),256,0,stream>>>(Ci,2048,x,640,1,2048,A2+640,1920,g);
  gemm_k<0><<<dim3(120,128),256,0,stream>>>(x,640,wqkv,1920,1,640,qkv,1920,g);
  attn_k<<<S,64,0,stream>>>(qkv,g,A2);
  final_k<<<dim3(40,128),256,0,stream>>>(A2,Wr,Wi,wo,(u16*)d_out);
}
```

```cpp
#include <hip/hip_runtime.h>
#include <hip/hip_bf16.h>

typedef unsigned short u16;
typedef unsigned u32;
typedef u32 u32x2 __attribute__((ext_vector_type(2)));
typedef u32 u32x4 __attribute__((ext_vector_type(4)));
typedef __bf16 bf16x2 __attribute__((ext_vector_type(2)));
typedef __bf16 bf16x4 __attribute__((ext_vector_type(4)));
typedef __bf16 bf16x8 __attribute__((ext_vector_type(8)));
typedef float f32x2 __attribute__((ext_vector_type(2)));
typedef float f32x4 __attribute__((ext_vector_type(4)));
#define LDS3 __attribute__((address_space(3)))
#define DEVI __device__ __forceinline__

#define STAG_W 4
#define STAG_X 4

template <int N> DEVI void waitv() { asm volatile("s_waitcnt vmcnt(%0)" ::"n"(N) : "memory"); }
DEVI void waitl0() { asm volatile("s_waitcnt lgkmcnt(0)" ::: "memory"); }
DEVI void bar() { asm volatile("" ::: "memory"); __builtin_amdgcn_s_barrier(); asm volatile("" ::: "memory"); }
DEVI void glds(const char* g, LDS3 char* l) { __builtin_amdgcn_global_load_lds((const u32*)g, (LDS3 u32*)l, 16, 0, 0); }
DEVI bf16x8 lds128(LDS3 char* p) { return *(LDS3 bf16x8*)p; }
DEVI bf16x4 lds64(LDS3 char* p) { return *(LDS3 bf16x4*)p; }
DEVI bf16x4 ldstr(LDS3 char* p) { return __builtin_amdgcn_ds_read_tr16_b64_v4bf16((LDS3 bf16x4*)p); }
DEVI bf16x8 cat(bf16x4 a, bf16x4 b) { return __builtin_shufflevector(a, b, 0, 1, 2, 3, 4, 5, 6, 7); }
DEVI f32x4 mfma(bf16x8 a, bf16x8 b, f32x4 c) { return __builtin_amdgcn_mfma_f32_16x16x32_bf16(a, b, c, 0, 0, 0); }
DEVI u32 pk2(float a, float b) { f32x2 v = {a, b}; bf16x2 r = __builtin_convertvector(v, bf16x2); return __builtin_bit_cast(u32, r); }
DEVI u32x2 pk4(f32x4 v) { return u32x2{pk2(v[0], v[1]), pk2(v[2], v[3])}; }
DEVI bf16x8 asb8(u32x2 a, u32x2 b) { u32x4 v = {a[0], a[1], b[0], b[1]}; return __builtin_bit_cast(bf16x8, v); }
DEVI float bflo(u32 v) { return __uint_as_float(v << 16); }
DEVI float bfhi(u32 v) { return __uint_as_float(v & 0xffff0000u); }
DEVI float xmax(float v) {
  auto a = __builtin_amdgcn_permlane16_swap(__float_as_uint(v), __float_as_uint(v), false, false);
  v = fmaxf(__uint_as_float(a[0]), __uint_as_float(a[1]));
  auto b = __builtin_amdgcn_permlane32_swap(__float_as_uint(v), __float_as_uint(v), false, false);
  return fmaxf(__uint_as_float(b[0]), __uint_as_float(b[1]));
}
DEVI float xsum(float v) {
  auto a = __builtin_amdgcn_permlane16_swap(__float_as_uint(v), __float_as_uint(v), false, false);
  v = __uint_as_float(a[0]) + __uint_as_float(a[1]);
  auto b = __builtin_amdgcn_permlane32_swap(__float_as_uint(v), __float_as_uint(v), false, false);
  return __uint_as_float(b[0]) + __uint_as_float(b[1]);
}

constexpr int A2_BYTES = 128 * 60 * 1024;
constexpr int K1_LDS = 139264;
constexpr int K2_LDS = 4 * 36864;

struct DFrags { bf16x8 x[4]; bf16x8 c[4]; };
template <int KK> DEVI void dft_read(DFrags& f, LDS3 char* ca, LDS3 char* const (&xb)[4]) {
#pragma unroll
  for (int tt = 0; tt < 4; tt++) f.c[tt] = lds128(ca + tt * 2048);
#pragma unroll
  for (int cc = 0; cc < 4; cc++) f.x[cc] = cat(ldstr(xb[cc] + KK * 8192), ldstr(xb[cc] + KK * 8192 + 1024));
}
DEVI void dft_mma(f32x4 (&acc)[4][4], const DFrags& f) {
#pragma unroll
  for (int cc = 0; cc < 4; cc++)
#pragma unroll
    for (int tt = 0; tt < 4; tt++) acc[cc][tt] = mfma(f.x[cc], f.c[tt], acc[cc][tt]);
}
DEVI void dft_sched() {
#pragma unroll
  for (int i = 0; i < 12; i++) { __builtin_amdgcn_sched_group_barrier(0x8, 1, 0); __builtin_amdgcn_sched_group_barrier(0x100, 1, 0); }
  __builtin_amdgcn_sched_group_barrier(0x8, 4, 0);
}

DEVI void dft_role(LDS3 char* smem, const char* Cr, const char* Ci, const char* x, char* A2, int xcd, int j, int w, int lane) {
  const int ml = j / 5, nblk = j % 5, part = ml >> 1, tb = 2 * xcd + (ml & 1);
  if (w >= 4) {
    const int lw = w - 4;
    const char* Cb = (part ? Ci : Cr) + (size_t)tb * 128 * 4096;
    const char* Xb = x + nblk * 256;
    u32 voA[4], voB[4];
#pragma unroll
    for (int i = 0; i < 4; i++) {
      const int p = lw * 4 + i;
      voA[i] = (8 * p + (lane >> 3)) * 4096 + 16 * ((lane & 7) ^ ((4 * p + (lane >> 4)) & 7));
      const int f = (lane >> 4) | (((p >> 1) & 1) << 2);
      voB[i] = (4 * p + (lane >> 4)) * 1280 + 16 * ((lane & 15) ^ (f << 1));
    }
    const int phi = ((ml + nblk) & 3) * (STAG_W / 4);
    auto issue = [&](int i) {
      const int ks = (((i & ~(STAG_W - 1)) | ((i + phi) & (STAG_W - 1))) + STAG_X * xcd) & 31;
      const char* ca = Cb + ks * 128;
      const char* xa = Xb + ks * 64 * 1280;
      LDS3 char* dst = smem + (i & 3) * 16384 + lw * 4096;
#pragma unroll
      for (int q = 0; q < 4; q++) glds(ca + voA[q], dst + q * 1024);
#pragma unroll
      for (int q = 0; q < 4; q++) glds(xa + voB[q], dst + 65536 + q * 1024);
    };
    issue(0); issue(1);
    waitv<8>(); bar();
    issue(2);
    waitv<8>(); bar();
    issue(3); issue(4);
    for (int t = 1; t < 28; t++) { waitv<16>(); bar(); issue(t + 4); }
    waitv<16>(); bar();
    waitv<8>(); bar();
    waitv<0>(); bar();
  } else {
    const int wt = w & 1, wd = w >> 1, g = lane >> 4, e = lane & 15;
    LDS3 char* const cA0 = smem + (wt * 64 + e) * 128 + 16 * (g ^ (e >> 1));
    const int q = e >> 2, p = lane & 3, F = q | ((g & 1) << 2);
    LDS3 char* const xrow = smem + 65536 + (8 * g + q) * 256 + 8 * p;
    LDS3 char* xb0[4];
#pragma unroll
    for (int c = 0; c < 4; c++) xb0[c] = xrow + 32 * ((4 * wd + c) ^ F);
    f32x4 acc[4][4];
#pragma unroll
    for (int a = 0; a < 4; a++)
#pragma unroll
      for (int b = 0; b < 4; b++) acc[a][b] = f32x4{0, 0, 0, 0};
    DFrags F0, F1;
    bar();
    dft_read<0>(F0, cA0, xb0);
    int so = 0;
    for (int t = 0; t < 31; t++) {
      {
        LDS3 char* xb[4] = {xb0[0] + so, xb0[1] + so, xb0[2] + so, xb0[3] + so};
        dft_read<1>(F1, (LDS3 char*)((u32)(size_t)(cA0 + so) ^ 64u), xb);
      }
      dft_mma(acc, F0); dft_sched();
      __builtin_amdgcn_sched_barrier(0);
      so = (so + 16384) & 0xffff;
      waitl0(); bar();
      __builtin_amdgcn_sched_barrier(0);
      {
        LDS3 char* xb[4] = {xb0[0] + so, xb0[1] + so, xb0[2] + so, xb0[3] + so};
        dft_read<0>(F0, cA0 + so, xb);
      }
      dft_mma(acc, F1); dft_sched();
      __builtin_amdgcn_sched_barrier(0);
    }
    {
      LDS3 char* xb[4] = {xb0[0] + so, xb0[1] + so, xb0[2] + so, xb0[3] + so};
      dft_read<1>(F1, (LDS3 char*)((u32)(size_t)(cA0 + so) ^ 64u), xb);
    }
    dft_mma(acc, F0); dft_sched();
    __builtin_amdgcn_sched_barrier(0);
    dft_mma(acc, F1);
    const u32 sg = part ? 0x80008000u : 0u;
#pragma unroll
    for (int tt = 0; tt < 4; tt++) {
      const int tblk = tb * 8 + wt * 4 + tt;
#pragma unroll
      for (int u = 0; u < 2; u++) {
        const int kblk = part * 20 + nblk * 4 + wd * 2 + u;
        u32x2 a = pk4(acc[2 * u][tt]), b = pk4(acc[2 * u + 1][tt]);
        u32x4 o = {a[0] ^ sg, a[1] ^ sg, b[0] ^ sg, b[1] ^ sg};
        *(u32x4*)(A2 + ((size_t)(tblk * 60 + kblk) * 64 + lane) * 16) = o;
      }
    }
  }
}

constexpr int QA_SLOT = 24576, QB0 = 3 * QA_SLOT, QB_SLOT = 16384;

DEVI void qkv_role(LDS3 char* smem, const char* x, const char* wqkv, const char* gw, char* A2, float* gbuf, int hg, int rb, int w, int lane) {
  const int R0 = 176 * rb - 16;
  if (w >= 4) {
    const int lw = w - 4;
    u32 voA[2][3], voB[4];
#pragma unroll
    for (int h = 0; h < 2; h++)
#pragma unroll
      for (int i = 0; i < 3; i++) {
        const int p = 12 * h + 3 * lw + i;
        const int gr = min(max(R0 + 8 * p + (lane >> 3), 0), 2047);
        voA[h][i] = gr * 1280 + 16 * ((lane & 7) ^ ((4 * p + (lane >> 4)) & 7));
      }
#pragma unroll
    for (int i = 0; i < 4; i++) {
      const int p = 4 * lw + i, row = 2 * p + (lane >> 5);
      const int F = (row & 3) | (((row >> 3) & 1) << 2);
      int ch = (lane & 31) ^ (F << 1);
      if (ch > 29) ch -= 2;
      const int seg = ch / 10, c = ch - seg * 10;
      voB[i] = row * 3840 + seg * 1280 + hg * 160 + c * 16;
    }
    auto issueA = [&](int n, int aslot) {
      const char* xa = x + (n >> 1) * 128;
      LDS3 char* dst = smem + aslot * QA_SLOT + ((n & 1) * 12 + 3 * lw) * 1024;
#pragma unroll
      for (int i = 0; i < 3; i++) glds(xa + ((n & 1) ? voA[1][i] : voA[0][i]), dst + i * 1024);
    };
    auto issueB = [&](int s) {
      const char* wa = wqkv + s * 32 * 3840;
      LDS3 char* dst = smem + QB0 + (s & 3) * QB_SLOT + lw * 4096;
#pragma unroll
      for (int i = 0; i < 4; i++) glds(wa + voB[i], dst + i * 1024);
    };
    issueA(0, 0);
    issueB(0); issueA(1, 0);
    issueB(1); issueA(2, 1);
    waitv<7>(); bar();
    issueB(2); issueA(3, 1);
    waitv<7>(); bar();
    issueB(3); issueA(4, 2);
    issueB(4); issueA(5, 2);
    int as = 0;
    for (int s = 1; s < 15; s += 2) {
      waitv<14>(); bar(); issueB(s + 4); issueA(s + 5, as);
      waitv<14>(); bar(); issueB(s + 5); issueA(s + 6, as);
      as = (as == 2) ? 0 : as + 1;
    }
    waitv<14>(); bar(); issueB(19);
    waitv<11>(); bar();
    waitv<4>(); bar();
    waitv<0>(); bar();
    {
      const int i0 = (rb * 8 + hg) * 22;
      const int e = lane & 15;
#pragma unroll
      for (int r = 0; r < 2; r++) {
        const int lr = r * 16 + lw * 4 + (lane >> 4);
        const int t = min(i0 + lr, 2047);
        float s = 0.f;
#pragma unroll
        for (int c = 0; c < 5; c++) {
          const u32x4 a = *(const u32x4*)(x + t * 1280 + (e + 16 * c) * 16);
          const u32x4 b = *(const u32x4*)(gw + (e + 16 * c) * 16);
#pragma unroll
          for (int k = 0; k < 4; k++) { s = fmaf(bflo(a[k]), bflo(b[k]), s); s = fmaf(bfhi(a[k]), bfhi(b[k]), s); }
        }
        s += __shfl_xor(s, 1); s += __shfl_xor(s, 2); s += __shfl_xor(s, 4); s += __shfl_xor(s, 8);
        if (e == 0 && lr < 22 && i0 + lr < 2048) gbuf[t] = 1.f / (1.f + __expf(-s));
      }
    }
    bar();
    bar();
  } else {
    const int g = lane >> 4, e = lane & 15;
    LDS3 char* const xa0 = smem + (48 * w + e) * 128 + 16 * (g ^ (e >> 1));
    const int q = e >> 2, p = lane & 3, F = q | ((g & 1) << 2);
    LDS3 char* const wrow = smem + QB0 + (8 * g + q) * 512 + 8 * p;
    LDS3 char* wb0[8];
#pragma unroll
    for (int c = 0; c < 8; c++) wb0[c] = wrow + 32 * (c ^ F);
    f32x4 acc[3][15];
#pragma unroll
    for (int a = 0; a < 3; a++)
#pragma unroll
      for (int b = 0; b < 15; b++) acc[a][b] = f32x4{0, 0, 0, 0};
    bf16x8 xf[3], xn[3], wf[15];
    auto rdW = [&](int c, int so) { LDS3 char* a = wb0[c & 7] + so + (c >> 3) * 256; return cat(ldstr(a), ldstr(a + 2048)); };
    auto mm = [&](int c) {
#pragma unroll
      for (int m = 0; m < 3; m++) acc[m][c] = (c < 10) ? mfma(wf[c], xf[m], acc[m][c]) : mfma(xf[m], wf[c], acc[m][c]);
    };
    bar();
#pragma unroll
    for (int m = 0; m < 3; m++) xf[m] = lds128(xa0 + m * 2048);
    wf[0] = rdW(0, 0); wf[1] = rdW(1, 0); wf[2] = rdW(2, 0);
    int sb = 0, sa = 0;
    for (int s = 0; s < 20; s++) {
#pragma unroll
      for (int c = 0; c < 12; c++) { wf[c + 3] = rdW(c + 3, sb); mm(c); }
#pragma unroll
      for (int c = 0; c < 12; c++) { __builtin_amdgcn_sched_group_barrier(0x8, 1, 0); __builtin_amdgcn_sched_group_barrier(0x100, 1, 0); __builtin_amdgcn_sched_group_barrier(0x8, 1, 0); __builtin_amdgcn_sched_group_barrier(0x100, 1, 0); __builtin_amdgcn_sched_group_barrier(0x8, 1, 0); }
      __builtin_amdgcn_sched_barrier(0);
      sb = (sb + QB_SLOT) & 0xffff;
      if (s & 1) { sa = sa - 64 + QA_SLOT; if (sa == 3 * QA_SLOT) sa = 0; } else sa += 64;
      if (s < 19) { waitl0(); bar(); }
      __builtin_amdgcn_sched_barrier(0);
      LDS3 char* xa = (LDS3 char*)(((u32)(size_t)xa0 ^ (u32)(sa & 64)) + (u32)(sa & ~64));
#pragma unroll
      for (int m = 0; m < 3; m++) xn[m] = lds128(xa + m * 2048);
      bf16x8 n0 = rdW(0, sb), n1 = rdW(1, sb), n2 = rdW(2, sb);
      mm(12); mm(13); mm(14);
#pragma unroll
      for (int c = 0; c < 9; c++) { __builtin_amdgcn_sched_group_barrier(0x8, 1, 0); __builtin_amdgcn_sched_group_barrier(0x100, 1, 0); }
      __builtin_amdgcn_sched_barrier(0);
      wf[0] = n0; wf[1] = n1; wf[2] = n2;
#pragma unroll
      for (int m = 0; m < 3; m++) xf[m] = xn[m];
    }
    u32x2 pk[3][15];
#pragma unroll
    for (int m = 0; m < 3; m++)
#pragma unroll
      for (int c = 0; c < 15; c++) pk[m][c] = pk4(acc[m][c]);
    waitl0(); bar();
#pragma unroll
    for (int c = 0; c < 10; c++) *(LDS3 u32x2*)(smem + ((w * 10 + c) * 64 + lane) * 8) = pk[2][5 + c];
    waitl0(); bar();
    u32x2 hk[10];
    const int wp = (w > 0) ? w - 1 : 0;
#pragma unroll
    for (int c = 0; c < 10; c++) hk[c] = *(LDS3 u32x2*)(smem + ((wp * 10 + c) * 64 + lane) * 8);
    const bool par = e & 1;
    const int k0 = 4 * g + (e & 1);
    const bool vc0 = k0 <= e, vc1 = k0 + 2 <= e;
    const u32 psh = (e & 1) * 16;
    const float cs = 0.22360679774997896f * 1.4426950408889634f;
    const float NEG = -3.0e38f;
#pragma unroll
    for (int m = 0; m < 3; m++) {
      const int tq = 11 * rb + 3 * w + m - 1;
      if (m == 0 && w == 0) continue;
      if (tq >= 128) continue;
      const bool vp0 = (k0 >= e + 2) && tq > 0, vp1 = (k0 + 2 >= e + 2) && tq > 0;
      f32x4 lo[4], hi[4];
#pragma unroll
      for (int h = 0; h < 4; h++) {
        const u32x2 z = {0u, 0u};
        const bf16x8 Q = asb8(g >= h ? pk[m][h] : z, g <= h ? pk[m][h + 1] : z);
        const bf16x8 Kc = asb8(pk[m][5 + h], pk[m][6 + h]);
        const bf16x8 Kp = (m == 0) ? asb8(hk[h], hk[h + 1]) : asb8(pk[m > 0 ? m - 1 : 0][5 + h], pk[m > 0 ? m - 1 : 0][6 + h]);
        const f32x4 Sp = mfma(Kp, Q, f32x4{0, 0, 0, 0});
        const f32x4 Sc = mfma(Kc, Q, f32x4{0, 0, 0, 0});
        float s0 = par ? Sp[1] : Sp[0], s1 = par ? Sp[3] : Sp[2], s2 = par ? Sc[1] : Sc[0], s3 = par ? Sc[3] : Sc[2];
        s0 = vp0 ? s0 : NEG; s1 = vp1 ? s1 : NEG; s2 = vc0 ? s2 : NEG; s3 = vc1 ? s3 : NEG;
        const float mx = xmax(fmaxf(fmaxf(s0, s1), fmaxf(s2, s3)));
        float p0 = __builtin_amdgcn_exp2f((s0 - mx) * cs), p1 = __builtin_amdgcn_exp2f((s1 - mx) * cs);
        float p2 = __builtin_amdgcn_exp2f((s2 - mx) * cs), p3 = __builtin_amdgcn_exp2f((s3 - mx) * cs);
        const float inv = __builtin_amdgcn_rcpf(xsum((p0 + p1) + (p2 + p3)));
        const u32x4 pv = {pk2(p0 * inv, 0.f) << psh, pk2(p1 * inv, 0.f) << psh, pk2(p2 * inv, 0.f) << psh, pk2(p3 * inv, 0.f) << psh};
        const bf16x8 P = __builtin_bit_cast(bf16x8, pv);
        const bf16x8 V0 = asb8((m == 0) ? hk[5 + h] : pk[m > 0 ? m - 1 : 0][10 + h], pk[m][10 + h]);
        const bf16x8 V1 = asb8((m == 0) ? hk[6 + h] : pk[m > 0 ? m - 1 : 0][11 + h], pk[m][11 + h]);
        lo[h] = mfma(V0, P, f32x4{0, 0, 0, 0});
        hi[h] = mfma(V1, P, f32x4{0, 0, 0, 0});
      }
      const u32x2 o0 = pk4(lo[0]), o1 = pk4(g >= 1 ? lo[1] : hi[0]), o2 = pk4(g >= 2 ? lo[2] : hi[1]), o3 = pk4(g >= 3 ? lo[3] : hi[2]), o4 = pk4(hi[3]);
      char* dst = A2 + (size_t)tq * 60 * 1024;
      *(u32x4*)(dst + (40 + 2 * hg) * 1024 + lane * 16) = u32x4{o0[0], o0[1], o1[0], o1[1]};
      *(u32x4*)(dst + (41 + 2 * hg) * 1024 + lane * 16) = u32x4{o2[0], o2[1], o3[0], o3[1]};
      *(u32x2*)(dst + (56 + (hg >> 1)) * 1024 + (hg & 1) * 512 + lane * 8) = o4;
    }
  }
}

__global__ __launch_bounds__(512, 2) void k1(const char* __restrict__ x, const char* __restrict__ Cr, const char* __restrict__ Ci,
                                             const char* __restrict__ wqkv, const char* __restrict__ gw, char* __restrict__ ws) {
  extern __shared__ __attribute__((aligned(16))) char smem_[];
  LDS3 char* smem = (LDS3 char*)smem_;
  const int bid = blockIdx.x, xcd = bid & 7, j = bid >> 3;
  const int w = __builtin_amdgcn_readfirstlane(threadIdx.x >> 6), lane = threadIdx.x & 63;
  if (j < 20) dft_role(smem, Cr, Ci, x, ws, xcd, j, w, lane);
  else qkv_role(smem, x, wqkv, gw, ws, (float*)(ws + A2_BYTES), xcd, j - 20, w, lane);
}

__global__ __launch_bounds__(256, 1) void k2(const char* __restrict__ ws, const char* __restrict__ Wr, const char* __restrict__ Wi,
                                             const char* __restrict__ wo, char* __restrict__ out) {
  extern __shared__ __attribute__((aligned(16))) char smem_[];
  const int bid = blockIdx.x, xcd = bid & 7, j = bid >> 3, mb = 4 * xcd + (j >> 3), nb = j & 7;
  const int w = __builtin_amdgcn_readfirstlane(threadIdx.x >> 6), lane = threadIdx.x & 63;
  LDS3 char* const sm = (LDS3 char*)smem_ + w * 36864;
  const int g = lane >> 4, e = lane & 15;
  const char* const A2 = ws + (size_t)mb * 4 * 60 * 1024 + lane * 16;
  u32 voW[2];
#pragma unroll
  for (int i = 0; i < 2; i++) voW[i] = (80 * nb + (lane >> 3)) * 1280 + 16 * ((lane & 7) ^ ((4 * i + (lane >> 4)) & 7));
  const int rot = (nb + mb) % 5;
  auto issueA = [&](int u, LDS3 char* dst) {
#pragma unroll
    for (int mt = 0; mt < 4; mt++) { glds(A2 + (mt * 60 + 2 * u) * 1024, dst + mt * 2048); glds(A2 + (mt * 60 + 2 * u + 1) * 1024, dst + mt * 2048 + 1024); }
  };
  auto issue = [&](int i) {
    LDS3 char* dst = sm + (i & 1) * 18432;
    if (i < 5) {
      int ii = i + rot; if (ii >= 5) ii -= 5;
      const int u = w + 4 * ii;
      const char* wb = (u < 10 ? Wr : Wi - 1280) + u * 128;
#pragma unroll
      for (int p = 0; p < 10; p++) glds(wb + p * 10240 + voW[p & 1], dst + 8192 + p * 1024);
      issueA(u, dst);
    } else {
      const int u = w + 4 * i;
      const bool tail = u >= 28;
#pragma unroll
      for (int p = 0; p < 10; p++) {
        const int n = 64 * p + lane, r = n / 10, c = n - r * 10;
        const int row = tail ? 80 * (4 * (u - 28) + (r >> 4)) + 64 + (r & 15) : 80 * (u - 20) + r;
        glds(wo + row * 1280 + nb * 160 + c * 16, dst + 8192 + p * 1024);
      }
      issueA(u, dst);
    }
  };
  const int nu = (w < 2) ? 8 : 7;
  f32x4 accP[4][5], accO[4][5];
#pragma unroll
  for (int a = 0; a < 4; a++)
#pragma unroll
    for (int b = 0; b < 5; b++) { accP[a][b] = f32x4{0, 0, 0, 0}; accO[a][b] = f32x4{0, 0, 0, 0}; }
  issue(0); issue(1);
  const u32 wo0 = 8192 + e * 128 + 8 * (g & 1), sw = e >> 1;
  for (int i = 0; i < 5; i++) {
    LDS3 char* s = sm + (i & 1) * 18432;
    waitv<18>();
    bf16x8 a[2][4], wf[2][5];
#pragma unroll
    for (int kb = 0; kb < 2; kb++) {
#pragma unroll
      for (int mt = 0; mt < 4; mt++) a[kb][mt] = lds128(s + mt * 2048 + kb * 1024 + lane * 16);
#pragma unroll
      for (int c = 0; c < 5; c++)
        wf[kb][c] = cat(lds64(s + wo0 + c * 2048 + 16 * ((4 * kb + (g >> 1)) ^ sw)), lds64(s + wo0 + c * 2048 + 16 * ((4 * kb + 2 + (g >> 1)) ^ sw)));
    }
    waitl0(); __builtin_amdgcn_sched_barrier(0);
    issue(i + 2);
    __builtin_amdgcn_sched_barrier(0);
#pragma unroll
    for (int kb = 0; kb < 2; kb++)
#pragma unroll
      for (int c = 0; c < 5; c++)
#pragma unroll
        for (int mt = 0; mt < 4; mt++) accP[mt][c] = mfma(wf[kb][c], a[kb][mt], accP[mt][c]);
  }
  const u32 to0 = 8192 + (4 * g + (e >> 2)) * 160 + 8 * (lane & 3);
  for (int i = 5; i < nu; i++) {
    LDS3 char* s = sm + (i & 1) * 18432;
    if (i + 1 < nu) waitv<18>(); else waitv<0>();
    const bool tail = (w + 4 * i) >= 28;
    const u32 a0 = tail ? lane * 8 : lane * 16, a1 = tail ? 512 + lane * 8 : lane * 16 + 8;
    bf16x8 a[2][4], wf[2][5];
#pragma unroll
    for (int kb = 0; kb < 2; kb++) {
#pragma unroll
      for (int mt = 0; mt < 4; mt++) a[kb][mt] = cat(lds64(s + mt * 2048 + kb * 1024 + a0), lds64(s + mt * 2048 + kb * 1024 + a1));
#pragma unroll
      for (int c = 0; c < 5; c++) wf[kb][c] = cat(ldstr(s + to0 + kb * 5120 + c * 32), ldstr(s + to0 + kb * 5120 + 2560 + c * 32));
    }
    waitl0(); __builtin_amdgcn_sched_barrier(0);
    if (i + 2 < nu) issue(i + 2);
    __builtin_amdgcn_sched_barrier(0);
#pragma unroll
    for (int kb = 0; kb < 2; kb++)
#pragma unroll
      for (int c = 0; c < 5; c++)
#pragma unroll
        for (int mt = 0; mt < 4; mt++) accO[mt][c] = mfma(wf[kb][c], a[kb][mt], accO[mt][c]);
  }
  const float* gb = (const float*)(ws + A2_BYTES) + mb * 64 + e;
  float gv[4];
#pragma unroll
  for (int mt = 0; mt < 4; mt++) gv[mt] = gb[mt * 16];
  __syncthreads();
  LDS3 char* const red = (LDS3 char*)smem_;
#pragma unroll
  for (int mt = 0; mt < 4; mt++)
#pragma unroll
    for (int c = 0; c < 5; c++) {
      const f32x4 v = accP[mt][c] * gv[mt] + accO[mt][c] * (1.f - gv[mt]);
      *(LDS3 f32x4*)(red + w * 21504 + (mt * 16 + e) * 336 + c * 64 + g * 16) = v;
    }
  __syncthreads();
  for (int n = threadIdx.x; n < 640; n += 256) {
    const int r = n / 10, c = n - r * 10;
    LDS3 char* p = red + r * 336 + c * 32;
    f32x4 s0 = *(LDS3 f32x4*)p, s1 = *(LDS3 f32x4*)(p + 16);
#pragma unroll
    for (int k = 1; k < 4; k++) { s0 += *(LDS3 f32x4*)(p + k * 21504); s1 += *(LDS3 f32x4*)(p + k * 21504 + 16); }
    const u32x2 a = pk4(s0), b = pk4(s1);
    *(u32x4*)(out + (size_t)(mb * 64 + r) * 1280 + nb * 160 + c * 16) = u32x4{a[0], a[1], b[0], b[1]};
  }
}

extern "C" void kernel_launch(void* const* d_in, const int* in_sizes, int n_in, void* d_out, int out_size, void* d_ws, size_t ws_size,
                              hipStream_t stream) {
  (void)hipFuncSetAttribute((const void*)k1, hipFuncAttributeMaxDynamicSharedMemorySize, K1_LDS);
  (void)hipFuncSetAttribute((const void*)k2, hipFuncAttributeMaxDynamicSharedMemorySize, K2_LDS);
  k1<<<256, 512, K1_LDS, stream>>>((const char*)d_in[0], (const char*)d_in[1], (const char*)d_in[2], (const char*)d_in[6],
                                   (const char*)d_in[8], (char*)d_ws);
  k2<<<256, 256, K2_LDS, stream>>>((const char*)d_ws, (const char*)d_in[3], (const char*)d_in[4], (const char*)d_in[7], (char*)d_out);
}
```

```cpp
#include <hip/hip_runtime.h>
#include <hip/hip_bf16.h>

typedef unsigned short u16;
typedef unsigned u32;
typedef u32 u32x2 __attribute__((ext_vector_type(2)));
typedef u32 u32x4 __attribute__((ext_vector_type(4)));
typedef __bf16 bf16x2 __attribute__((ext_vector_type(2)));
typedef __bf16 bf16x4 __attribute__((ext_vector_type(4)));
typedef __bf16 bf16x8 __attribute__((ext_vector_type(8)));
typedef float f32x2 __attribute__((ext_vector_type(2)));
typedef float f32x4 __attribute__((ext_vector_type(4)));
#define LDS3 __attribute__((address_space(3)))
#define DEVI __device__ __forceinline__

#define STAG_W 4
#define STAG_X 4

template <int N> DEVI void waitv() { asm volatile("s_waitcnt vmcnt(%0)" ::"n"(N) : "memory"); }
DEVI void waitl0() { asm volatile("s_waitcnt lgkmcnt(0)" ::: "memory"); }
DEVI void bar() { asm volatile("" ::: "memory"); __builtin_amdgcn_s_barrier(); asm volatile("" ::: "memory"); }
DEVI void glds(const char* g, LDS3 char* l) { __builtin_amdgcn_global_load_lds((const u32*)g, (LDS3 u32*)l, 16, 0, 0); }
DEVI bf16x8 lds128(LDS3 char* p) { return *(LDS3 bf16x8*)p; }
DEVI bf16x4 lds64(LDS3 char* p) { return *(LDS3 bf16x4*)p; }
DEVI bf16x4 ldstr(LDS3 char* p) { return __builtin_amdgcn_ds_read_tr16_b64_v4bf16((LDS3 bf16x4*)p); }
DEVI bf16x8 cat(bf16x4 a, bf16x4 b) { return __builtin_shufflevector(a, b, 0, 1, 2, 3, 4, 5, 6, 7); }
DEVI f32x4 mfma(bf16x8 a, bf16x8 b, f32x4 c) { return __builtin_amdgcn_mfma_f32_16x16x32_bf16(a, b, c, 0, 0, 0); }
DEVI u32 pk2(float a, float b) { f32x2 v = {a, b}; bf16x2 r = __builtin_convertvector(v, bf16x2); return __builtin_bit_cast(u32, r); }
DEVI u32x2 pk4(f32x4 v) { return u32x2{pk2(v[0], v[1]), pk2(v[2], v[3])}; }
DEVI bf16x8 asb8(u32x2 a, u32x2 b) { u32x4 v = {a[0], a[1], b[0], b[1]}; return __builtin_bit_cast(bf16x8, v); }
DEVI float bflo(u32 v) { return __uint_as_float(v << 16); }
DEVI float bfhi(u32 v) { return __uint_as_float(v & 0xffff0000u); }
DEVI float xmax(float v) {
  auto a = __builtin_amdgcn_permlane16_swap(__float_as_uint(v), __float_as_uint(v), false, false);
  v = fmaxf(__uint_as_float(a[0]), __uint_as_float(a[1]));
  auto b = __builtin_amdgcn_permlane32_swap(__float_as_uint(v), __float_as_uint(v), false, false);
  return fmaxf(__uint_as_float(b[0]), __uint_as_float(b[1]));
}
DEVI float xsum(float v) {
  auto a = __builtin_amdgcn_permlane16_swap(__float_as_uint(v), __float_as_uint(v), false, false);
  v = __uint_as_float(a[0]) + __uint_as_float(a[1]);
  auto b = __builtin_amdgcn_permlane32_swap(__float_as_uint(v), __float_as_uint(v), false, false);
  return __uint_as_float(b[0]) + __uint_as_float(b[1]);
}

constexpr int A2_BYTES = 128 * 60 * 1024;
constexpr int K1_LDS = 139264;
constexpr int K2_LDS = 4 * 36864;

struct DFrags { bf16x8 x[4]; bf16x8 c[4]; };
template <int KK> DEVI void dft_read(DFrags& f, LDS3 char* ca, LDS3 char* const (&xb)[4]) {
#pragma unroll
  for (int tt = 0; tt < 4; tt++) f.c[tt] = lds128(ca + tt * 2048);
#pragma unroll
  for (int cc = 0; cc < 4; cc++) f.x[cc] = cat(ldstr(xb[cc] + KK * 8192), ldstr(xb[cc] + KK * 8192 + 1024));
}
DEVI void dft_mma(f32x4 (&acc)[4][4], const DFrags& f) {
#pragma unroll
  for (int cc = 0; cc < 4; cc++)
#pragma unroll
    for (int tt = 0; tt < 4; tt++) acc[cc][tt] = mfma(f.x[cc], f.c[tt], acc[cc][tt]);
}
DEVI void dft_sched() {
#pragma unroll
  for (int i = 0; i < 12; i++) { __builtin_amdgcn_sched_group_barrier(0x8, 1, 0); __builtin_amdgcn_sched_group_barrier(0x100, 1, 0); }
  __builtin_amdgcn_sched_group_barrier(0x8, 4, 0);
}

DEVI void dft_role(LDS3 char* smem, const char* Cr, const char* Ci, const char* x, char* A2, int xcd, int j, int w, int lane) {
  const int ml = j / 5, nblk = j % 5, part = ml >> 1, tb = 2 * xcd + (ml & 1);
  if (w >= 4) {
    const int lw = w - 4;
    const char* Cb = (part ? Ci : Cr) + (size_t)tb * 128 * 4096;
    const char* Xb = x + nblk * 256;
    u32 voA[4], voB[4];
#pragma unroll
    for (int i = 0; i < 4; i++) {
      const int p = lw * 4 + i;
      voA[i] = (8 * p + (lane >> 3)) * 4096 + 16 * ((lane & 7) ^ ((4 * p + (lane >> 4)) & 7));
      const int f = (lane >> 4) | (((p >> 1) & 1) << 2);
      voB[i] = (4 * p + (lane >> 4)) * 1280 + 16 * ((lane & 15) ^ (f << 1));
    }
    const int phi = ((ml + nblk) & 3) * (STAG_W / 4);
    auto issue = [&](int i) {
      const int ks = (((i & ~(STAG_W - 1)) | ((i + phi) & (STAG_W - 1))) + STAG_X * xcd) & 31;
      const char* ca = Cb + ks * 128;
      const char* xa = Xb + ks * 64 * 1280;
      LDS3 char* dst = smem + (i & 3) * 16384 + lw * 4096;
#pragma unroll
      for (int q = 0; q < 4; q++) glds(ca + voA[q], dst + q * 1024);
#pragma unroll
      for (int q = 0; q < 4; q++) glds(xa + voB[q], dst + 65536 + q * 1024);
    };
    issue(0); issue(1);
    waitv<8>(); bar();
    issue(2);
    waitv<8>(); bar();
    issue(3); issue(4);
    for (int t = 1; t < 28; t++) { waitv<16>(); bar(); issue(t + 4); }
    waitv<16>(); bar();
    waitv<8>(); bar();
    waitv<0>(); bar();
  } else {
    const int wt = w & 1, wd = w >> 1, g = lane >> 4, e = lane & 15;
    LDS3 char* const cA0 = smem + (wt * 64 + e) * 128 + 16 * (g ^ (e >> 1));
    const int q = e >> 2, p = lane & 3, F = q | ((g & 1) << 2);
    LDS3 char* const xrow = smem + 65536 + (8 * g + q) * 256 + 8 * p;
    LDS3 char* xb0[4];
#pragma unroll
    for (int c = 0; c < 4; c++) xb0[c] = xrow + 32 * ((4 * wd + c) ^ F);
    f32x4 acc[4][4];
#pragma unroll
    for (int a = 0; a < 4; a++)
#pragma unroll
      for (int b = 0; b < 4; b++) acc[a][b] = f32x4{0, 0, 0, 0};
    DFrags F0, F1;
    bar();
    dft_read<0>(F0, cA0, xb0);
    int so = 0;
    for (int t = 0; t < 31; t++) {
      {
        LDS3 char* xb[4] = {xb0[0] + so, xb0[1] + so, xb0[2] + so, xb0[3] + so};
        dft_read<1>(F1, (LDS3 char*)((u32)(size_t)(cA0 + so) ^ 64u), xb);
      }
      dft_mma(acc, F0); dft_sched();
      __builtin_amdgcn_sched_barrier(0);
      so = (so + 16384) & 0xffff;
      waitl0(); bar();
      __builtin_amdgcn_sched_barrier(0);
      {
        LDS3 char* xb[4] = {xb0[0] + so, xb0[1] + so, xb0[2] + so, xb0[3] + so};
        dft_read<0>(F0, cA0 + so, xb);
      }
      dft_mma(acc, F1); dft_sched();
      __builtin_amdgcn_sched_barrier(0);
    }
    {
      LDS3 char* xb[4] = {xb0[0] + so, xb0[1] + so, xb0[2] + so, xb0[3] + so};
      dft_read<1>(F1, (LDS3 char*)((u32)(size_t)(cA0 + so) ^ 64u), xb);
    }
    dft_mma(acc, F0); dft_sched();
    __builtin_amdgcn_sched_barrier(0);
    dft_mma(acc, F1);
    const u32 sg = part ? 0x80008000u : 0u;
#pragma unroll
    for (int tt = 0; tt < 4; tt++) {
      const int tblk = tb * 8 + wt * 4 + tt;
#pragma unroll
      for (int u = 0; u < 2; u++) {
        const int kblk = part * 20 + nblk * 4 + wd * 2 + u;
        u32x2 a = pk4(acc[2 * u][tt]), b = pk4(acc[2 * u + 1][tt]);
        u32x4 o = {a[0] ^ sg, a[1] ^ sg, b[0] ^ sg, b[1] ^ sg};
        *(u32x4*)(A2 + ((size_t)(tblk * 60 + kblk) * 64 + lane) * 16) = o;
      }
    }
  }
}

constexpr int QA_SLOT = 24576, QB0 = 3 * QA_SLOT, QB_SLOT = 16384;

DEVI void qkv_role(LDS3 char* smem, const char* x, const char* wqkv, const char* gw, char* A2, float* gbuf, int hg, int rb, int w, int lane) {
  const int R0 = 176 * rb - 16;
  if (w >= 4) {
    const int lw = w - 4;
    u32 voA[2][3], voB[4];
#pragma unroll
    for (int h = 0; h < 2; h++)
#pragma unroll
      for (int i = 0; i < 3; i++) {
        const int p = 12 * h + 3 * lw + i;
        const int gr = min(max(R0 + 8 * p + (lane >> 3), 0), 2047);
        voA[h][i] = gr * 1280 + 16 * ((lane & 7) ^ ((4 * p + (lane >> 4)) & 7));
      }
#pragma unroll
    for (int i = 0; i < 4; i++) {
      const int p = 4 * lw + i, row = 2 * p + (lane >> 5);
      const int F = (row & 3) | (((row >> 3) & 1) << 2);
      int ch = (lane & 31) ^ (F << 1);
      if (ch > 29) ch -= 2;
      const int seg = ch / 10, c = ch - seg * 10;
      voB[i] = row * 3840 + seg * 1280 + hg * 160 + c * 16;
    }
    auto issueA = [&](int n, int aslot) {
      const char* xa = x + (n >> 1) * 128;
      LDS3 char* dst = smem + aslot * QA_SLOT + ((n & 1) * 12 + 3 * lw) * 1024;
#pragma unroll
      for (int i = 0; i < 3; i++) glds(xa + ((n & 1) ? voA[1][i] : voA[0][i]), dst + i * 1024);
    };
    auto issueB = [&](int s) {
      const char* wa = wqkv + s * 32 * 3840;
      LDS3 char* dst = smem + QB0 + (s & 3) * QB_SLOT + lw * 4096;
#pragma unroll
      for (int i = 0; i < 4; i++) glds(wa + voB[i], dst + i * 1024);
    };
    issueA(0, 0);
    issueB(0); issueA(1, 0);
    issueB(1); issueA(2, 1);
    waitv<7>(); bar();
    issueB(2); issueA(3, 1);
    waitv<7>(); bar();
    issueB(3); issueA(4, 2);
    issueB(4); issueA(5, 2);
    int as = 0;
    for (int s = 1; s < 15; s += 2) {
      waitv<14>(); bar(); issueB(s + 4); issueA(s + 5, as);
      waitv<14>(); bar(); issueB(s + 5); issueA(s + 6, as);
      as = (as == 2) ? 0 : as + 1;
    }
    waitv<14>(); bar(); issueB(19);
    waitv<11>(); bar();
    waitv<4>(); bar();
    waitv<0>(); bar();
    {
      const int i0 = (rb * 8 + hg) * 22;
      const int e = lane & 15;
#pragma unroll
      for (int r = 0; r < 2; r++) {
        const int lr = r * 16 + lw * 4 + (lane >> 4);
        const int t = min(i0 + lr, 2047);
        float s = 0.f;
#pragma unroll
        for (int c = 0; c < 5; c++) {
          const u32x4 a = *(const u32x4*)(x + t * 1280 + (e + 16 * c) * 16);
          const u32x4 b = *(const u32x4*)(gw + (e + 16 * c) * 16);
#pragma unroll
          for (int k = 0; k < 4; k++) { s = fmaf(bflo(a[k]), bflo(b[k]), s); s = fmaf(bfhi(a[k]), bfhi(b[k]), s); }
        }
        s += __shfl_xor(s, 1); s += __shfl_xor(s, 2); s += __shfl_xor(s, 4); s += __shfl_xor(s, 8);
        if (e == 0 && lr < 22 && i0 + lr < 2048) gbuf[t] = 1.f / (1.f + __expf(-s));
      }
    }
    bar();
    bar();
  } else {
    const int g = lane >> 4, e = lane & 15;
    LDS3 char* const xa0 = smem + (48 * w + e) * 128 + 16 * (g ^ (e >> 1));
    const int q = e >> 2, p = lane & 3, F = q | ((g & 1) << 2);
    LDS3 char* const wrow = smem + QB0 + (8 * g + q) * 512 + 8 * p;
    LDS3 char* wb0[8];
#pragma unroll
    for (int c = 0; c < 8; c++) wb0[c] = wrow + 32 * (c ^ F);
    f32x4 acc[3][15];
#pragma unroll
    for (int a = 0; a < 3; a++)
#pragma unroll
      for (int b = 0; b < 15; b++) acc[a][b] = f32x4{0, 0, 0, 0};
    bf16x8 xf[3], xn[3], wf[15];
    auto rdW = [&](int c, int so) { LDS3 char* a = wb0[c & 7] + so + (c >> 3) * 256; return cat(ldstr(a), ldstr(a + 2048)); };
    auto mm = [&](int c) {
#pragma unroll
      for (int m = 0; m < 3; m++) acc[m][c] = (c < 10) ? mfma(wf[c], xf[m], acc[m][c]) : mfma(xf[m], wf[c], acc[m][c]);
    };
    bar();
#pragma unroll
    for (int m = 0; m < 3; m++) xf[m] = lds128(xa0 + m * 2048);
    wf[0] = rdW(0, 0); wf[1] = rdW(1, 0); wf[2] = rdW(2, 0);
    int sb = 0, sa = 0;
    for (int s = 0; s < 20; s++) {
#pragma unroll
      for (int c = 0; c < 12; c++) { wf[c + 3] = rdW(c + 3, sb); mm(c); }
#pragma unroll
      for (int c = 0; c < 12; c++) { __builtin_amdgcn_sched_group_barrier(0x8, 1, 0); __builtin_amdgcn_sched_group_barrier(0x100, 1, 0); __builtin_amdgcn_sched_group_barrier(0x8, 1, 0); __builtin_amdgcn_sched_group_barrier(0x100, 1, 0); __builtin_amdgcn_sched_group_barrier(0x8, 1, 0); }
      __builtin_amdgcn_sched_barrier(0);
      sb = (sb + QB_SLOT) & 0xffff;
      if (s & 1) { sa = sa - 64 + QA_SLOT; if (sa == 3 * QA_SLOT) sa = 0; } else sa += 64;
      if (s < 19) { waitl0(); bar(); }
      __builtin_amdgcn_sched_barrier(0);
      LDS3 char* xa = (LDS3 char*)(((u32)(size_t)xa0 ^ (u32)(sa & 64)) + (u32)(sa & ~64));
#pragma unroll
      for (int m = 0; m < 3; m++) xn[m] = lds128(xa + m * 2048);
      bf16x8 n0 = rdW(0, sb), n1 = rdW(1, sb), n2 = rdW(2, sb);
      mm(12); mm(13); mm(14);
#pragma unroll
      for (int c = 0; c < 9; c++) { __builtin_amdgcn_sched_group_barrier(0x8, 1, 0); __builtin_amdgcn_sched_group_barrier(0x100, 1, 0); }
      __builtin_amdgcn_sched_barrier(0);
      wf[0] = n0; wf[1] = n1; wf[2] = n2;
#pragma unroll
      for (int m = 0; m < 3; m++) xf[m] = xn[m];
    }
    u32x2 pk[3][15];
#pragma unroll
    for (int m = 0; m < 3; m++)
#pragma unroll
      for (int c = 0; c < 15; c++) pk[m][c] = pk4(acc[m][c]);
    waitl0(); bar();
#pragma unroll
    for (int c = 0; c < 10; c++) *(LDS3 u32x2*)(smem + ((w * 10 + c) * 64 + lane) * 8) = pk[2][5 + c];
    waitl0(); bar();
    u32x2 hk[10];
    const int wp = (w > 0) ? w - 1 : 0;
#pragma unroll
    for (int c = 0; c < 10; c++) hk[c] = *(LDS3 u32x2*)(smem + ((wp * 10 + c) * 64 + lane) * 8);
    const bool par = e & 1;
    const int k0 = 4 * g + (e & 1);
    const bool vc0 = k0 <= e, vc1 = k0 + 2 <= e;
    const u32 psh = (e & 1) * 16;
    const float cs = 0.22360679774997896f * 1.4426950408889634f;
    const float NEG = -3.0e38f;
#pragma unroll
    for (int m = 0; m < 3; m++) {
      const int tq = 11 * rb + 3 * w + m - 1;
      if (m == 0 && w == 0) continue;
      if (tq >= 128) continue;
      const bool vp0 = (k0 >= e + 2) && tq > 0, vp1 = (k0 + 2 >= e + 2) && tq > 0;
      f32x4 lo[4], hi[4];
#pragma unroll
      for (int h = 0; h < 4; h++) {
        const u32x2 z = {0u, 0u};
        const bf16x8 Q = asb8(g >= h ? pk[m][h] : z, g <= h ? pk[m][h + 1] : z);
        const bf16x8 Kc = asb8(pk[m][5 + h], pk[m][6 + h]);
        const bf16x8 Kp = (m == 0) ? asb8(hk[h], hk[h + 1]) : asb8(pk[m > 0 ? m - 1 : 0][5 + h], pk[m > 0 ? m - 1 : 0][6 + h]);
        const f32x4 Sp = mfma(Kp, Q, f32x4{0, 0, 0, 0});
        const f32x4 Sc = mfma(Kc, Q, f32x4{0, 0, 0, 0});
        float s0 = par ? Sp[1] : Sp[0], s1 = par ? Sp[3] : Sp[2], s2 = par ? Sc[1] : Sc[0], s3 = par ? Sc[3] : Sc[2];
        s0 = vp0 ? s0 : NEG; s1 = vp1 ? s1 : NEG; s2 = vc0 ? s2 : NEG; s3 = vc1 ? s3 : NEG;
        const float mx = xmax(fmaxf(fmaxf(s0, s1), fmaxf(s2, s3)));
        float p0 = __builtin_amdgcn_exp2f((s0 - mx) * cs), p1 = __builtin_amdgcn_exp2f((s1 - mx) * cs);
        float p2 = __builtin_amdgcn_exp2f((s2 - mx) * cs), p3 = __builtin_amdgcn_exp2f((s3 - mx) * cs);
        const float inv = __builtin_amdgcn_rcpf(xsum((p0 + p1) + (p2 + p3)));
        const u32x4 pv = {pk2(p0 * inv, 0.f) << psh, pk2(p1 * inv, 0.f) << psh, pk2(p2 * inv, 0.f) << psh, pk2(p3 * inv, 0.f) << psh};
        const bf16x8 P = __builtin_bit_cast(bf16x8, pv);
        const bf16x8 V0 = asb8((m == 0) ? hk[5 + h] : pk[m > 0 ? m - 1 : 0][10 + h], pk[m][10 + h]);
        const bf16x8 V1 = asb8((m == 0) ? hk[6 + h] : pk[m > 0 ? m - 1 : 0][11 + h], pk[m][11 + h]);
        lo[h] = mfma(V0, P, f32x4{0, 0, 0, 0});
        hi[h] = mfma(V1, P, f32x4{0, 0, 0, 0});
      }
      const u32x2 o0 = pk4(lo[0]), o1 = pk4(g >= 1 ? lo[1] : hi[0]), o2 = pk4(g >= 2 ? lo[2] : hi[1]), o3 = pk4(g >= 3 ? lo[3] : hi[2]), o4 = pk4(hi[3]);
      char* dst = A2 + (size_t)tq * 60 * 1024;
      *(u32x4*)(dst + (40 + 2 * hg) * 1024 + lane * 16) = u32x4{o0[0], o0[1], o1[0], o1[1]};
      *(u32x4*)(dst + (41 + 2 * hg) * 1024 + lane * 16) = u32x4{o2[0], o2[1], o3[0], o3[1]};
      *(u32x2*)(dst + (56 + (hg >> 1)) * 1024 + (hg & 1) * 512 + lane * 8) = o4;
    }
  }
}

__global__ __launch_bounds__(512, 2) void k1(const char* __restrict__ x, const char* __restrict__ Cr, const char* __restrict__ Ci,
                                             const char* __restrict__ wqkv, const char* __restrict__ gw, char* __restrict__ ws) {
  extern __shared__ __attribute__((aligned(16))) char smem_[];
  LDS3 char* smem = (LDS3 char*)smem_;
  const int bid = blockIdx.x, xcd = bid & 7, j = bid >> 3;
  const int w = __builtin_amdgcn_readfirstlane(threadIdx.x >> 6), lane = threadIdx.x & 63;
  if (bid == 0 && threadIdx.x == 0) *(volatile unsigned long long*)(ws + (16u << 20)) = __builtin_amdgcn_s_memrealtime();
  if (j < 20) dft_role(smem, Cr, Ci, x, ws, xcd, j, w, lane);
  else qkv_role(smem, x, wqkv, gw, ws, (float*)(ws + A2_BYTES), xcd, j - 20, w, lane);
}

__global__ __launch_bounds__(256, 1) void k2(const char* __restrict__ ws, const char* __restrict__ Wr, const char* __restrict__ Wi,
                                             const char* __restrict__ wo, char* __restrict__ out) {
  extern __shared__ __attribute__((aligned(16))) char smem_[];
  const int bid = blockIdx.x, xcd = bid & 7, j = bid >> 3, mb = 4 * xcd + (j >> 3), nb = j & 7;
  const int w = __builtin_amdgcn_readfirstlane(threadIdx.x >> 6), lane = threadIdx.x & 63;
  LDS3 char* const sm = (LDS3 char*)smem_ + w * 36864;
  const int g = lane >> 4, e = lane & 15;
  const char* const A2 = ws + (size_t)mb * 4 * 60 * 1024 + lane * 16;
  u32 voW[2];
#pragma unroll
  for (int i = 0; i < 2; i++) voW[i] = (80 * nb + (lane >> 3)) * 1280 + 16 * ((lane & 7) ^ ((4 * i + (lane >> 4)) & 7));
  const int rot = (nb + mb) % 5;
  auto issueA = [&](int u, LDS3 char* dst) {
#pragma unroll
    for (int mt = 0; mt < 4; mt++) { glds(A2 + (mt * 60 + 2 * u) * 1024, dst + mt * 2048); glds(A2 + (mt * 60 + 2 * u + 1) * 1024, dst + mt * 2048 + 1024); }
  };
  auto issue = [&](int i) {
    LDS3 char* dst = sm + (i & 1) * 18432;
    if (i < 5) {
      int ii = i + rot; if (ii >= 5) ii -= 5;
      const int u = w + 4 * ii;
      const char* wb = (u < 10 ? Wr : Wi - 1280) + u * 128;
#pragma unroll
      for (int p = 0; p < 10; p++) glds(wb + p * 10240 + voW[p & 1], dst + 8192 + p * 1024);
      issueA(u, dst);
    } else {
      const int u = w + 4 * i;
      const bool tail = u >= 28;
#pragma unroll
      for (int p = 0; p < 10; p++) {
        const int n = 64 * p + lane, r = n / 10, c = n - r * 10;
        const int row = tail ? 80 * (4 * (u - 28) + (r >> 4)) + 64 + (r & 15) : 80 * (u - 20) + r;
        glds(wo + row * 1280 + nb * 160 + c * 16, dst + 8192 + p * 1024);
      }
      issueA(u, dst);
    }
  };
  const int nu = (w < 2) ? 8 : 7;
  f32x4 accP[4][5], accO[4][5];
#pragma unroll
  for (int a = 0; a < 4; a++)
#pragma unroll
    for (int b = 0; b < 5; b++) { accP[a][b] = f32x4{0, 0, 0, 0}; accO[a][b] = f32x4{0, 0, 0, 0}; }
  issue(0); issue(1);
  const u32 wo0 = 8192 + e * 128 + 8 * (g & 1), sw = e >> 1;
  for (int i = 0; i < 5; i++) {
    LDS3 char* s = sm + (i & 1) * 18432;
    waitv<18>();
    bf16x8 a[2][4], wf[2][5];
#pragma unroll
    for (int kb = 0; kb < 2; kb++) {
#pragma unroll
      for (int mt = 0; mt < 4; mt++) a[kb][mt] = lds128(s + mt * 2048 + kb * 1024 + lane * 16);
#pragma unroll
      for (int c = 0; c < 5; c++)
        wf[kb][c] = cat(lds64(s + wo0 + c * 2048 + 16 * ((4 * kb + (g >> 1)) ^ sw)), lds64(s + wo0 + c * 2048 + 16 * ((4 * kb + 2 + (g >> 1)) ^ sw)));
    }
    waitl0(); __builtin_amdgcn_sched_barrier(0);
    issue(i + 2);
    __builtin_amdgcn_sched_barrier(0);
#pragma unroll
    for (int kb = 0; kb < 2; kb++)
#pragma unroll
      for (int c = 0; c < 5; c++)
#pragma unroll
        for (int mt = 0; mt < 4; mt++) accP[mt][c] = mfma(wf[kb][c], a[kb][mt], accP[mt][c]);
  }
  const u32 to0 = 8192 + (4 * g + (e >> 2)) * 160 + 8 * (lane & 3);
  for (int i = 5; i < nu; i++) {
    LDS3 char* s = sm + (i & 1) * 18432;
    if (i + 1 < nu) waitv<18>(); else waitv<0>();
    const bool tail = (w + 4 * i) >= 28;
    const u32 a0 = tail ? lane * 8 : lane * 16, a1 = tail ? 512 + lane * 8 : lane * 16 + 8;
    bf16x8 a[2][4], wf[2][5];
#pragma unroll
    for (int kb = 0; kb < 2; kb++) {
#pragma unroll
      for (int mt = 0; mt < 4; mt++) a[kb][mt] = cat(lds64(s + mt * 2048 + kb * 1024 + a0), lds64(s + mt * 2048 + kb * 1024 + a1));
#pragma unroll
      for (int c = 0; c < 5; c++) wf[kb][c] = cat(ldstr(s + to0 + kb * 5120 + c * 32), ldstr(s + to0 + kb * 5120 + 2560 + c * 32));
    }
    waitl0(); __builtin_amdgcn_sched_barrier(0);
    if (i + 2 < nu) issue(i + 2);
    __builtin_amdgcn_sched_barrier(0);
#pragma unroll
    for (int kb = 0; kb < 2; kb++)
#pragma unroll
      for (int c = 0; c < 5; c++)
#pragma unroll
        for (int mt = 0; mt < 4; mt++) accO[mt][c] = mfma(wf[kb][c], a[kb][mt], accO[mt][c]);
  }
  const float* gb = (const float*)(ws + A2_BYTES) + mb * 64 + e;
  float gv[4];
#pragma unroll
  for (int mt = 0; mt < 4; mt++) gv[mt] = gb[mt * 16];
  __syncthreads();
  LDS3 char* const red = (LDS3 char*)smem_;
#pragma unroll
  for (int mt = 0; mt < 4; mt++)
#pragma unroll
    for (int c = 0; c < 5; c++) {
      const f32x4 v = accP[mt][c] * gv[mt] + accO[mt][c] * (1.f - gv[mt]);
      *(LDS3 f32x4*)(red + w * 21504 + (mt * 16 + e) * 336 + c * 64 + g * 16) = v;
    }
  __syncthreads();
  for (int n = threadIdx.x; n < 640; n += 256) {
    const int r = n / 10, c = n - r * 10;
    LDS3 char* p = red + r * 336 + c * 32;
    f32x4 s0 = *(LDS3 f32x4*)p, s1 = *(LDS3 f32x4*)(p + 16);
#pragma unroll
    for (int k = 1; k < 4; k++) { s0 += *(LDS3 f32x4*)(p + k * 21504); s1 += *(LDS3 f32x4*)(p + k * 21504 + 16); }
    const u32x2 a = pk4(s0), b = pk4(s1);
    *(u32x4*)(out + (size_t)(mb * 64 + r) * 1280 + nb * 160 + c * 16) = u32x4{a[0], a[1], b[0], b[1]};
  }
}

__global__ void k3(char* ws) { if (threadIdx.x == 0) { const unsigned long long t0 = *(volatile const unsigned long long*)(ws + (16u << 20));
    for (int i = 0; i < 100000; i++) { if (__builtin_amdgcn_s_memrealtime() - t0 >= 6000ull) break; __builtin_amdgcn_s_sleep(1); } } }
extern "C" void kernel_launch(void* const* d_in, const int* in_sizes, int n_in, void* d_out, int out_size, void* d_ws, size_t ws_size,
                              hipStream_t stream) {
  (void)hipFuncSetAttribute((const void*)k1, hipFuncAttributeMaxDynamicSharedMemorySize, K1_LDS);
  (void)hipFuncSetAttribute((const void*)k2, hipFuncAttributeMaxDynamicSharedMemorySize, K2_LDS);
  k1<<<256, 512, K1_LDS, stream>>>((const char*)d_in[0], (const char*)d_in[1], (const char*)d_in[2], (const char*)d_in[6],
                                   (const char*)d_in[8], (char*)d_ws);
  k2<<<256, 256, K2_LDS, stream>>>((const char*)d_ws, (const char*)d_in[3], (const char*)d_in[4], (const char*)d_in[7], (char*)d_out);
  k3<<<1, 64, 0, stream>>>((char*)d_ws);
}
```

```cpp
#define NO_RB 1
#include <hip/hip_runtime.h>
#include <hip/hip_bf16.h>

typedef unsigned u32;
typedef unsigned long long u64;
typedef u32 u32x2 __attribute__((ext_vector_type(2)));
typedef u32 u32x4 __attribute__((ext_vector_type(4)));
typedef u32 u32x8 __attribute__((ext_vector_type(8)));
typedef u32 u32x10 __attribute__((ext_vector_type(10)));
typedef __bf16 bf16x2 __attribute__((ext_vector_type(2)));
typedef __bf16 bf16x4 __attribute__((ext_vector_type(4)));
typedef __bf16 bf16x8 __attribute__((ext_vector_type(8)));
typedef float f32x2 __attribute__((ext_vector_type(2)));
typedef float f32x4 __attribute__((ext_vector_type(4)));
typedef float f32x16 __attribute__((ext_vector_type(16)));
typedef float f32x32 __attribute__((ext_vector_type(32)));
#define LDS3 __attribute__((address_space(3)))
#define GLB1 __attribute__((address_space(1)))
#define DEVI __device__ __forceinline__
#define SCHEDB() __builtin_amdgcn_sched_barrier(0)
#define SGB(m, n) __builtin_amdgcn_sched_group_barrier(m, n, 0)

constexpr u32 A2_BYTES = 128 * 60 * 1024;
constexpr u32 WS_EPOCH = 8u << 20;
constexpr u32 WS_FLAG = WS_EPOCH + 1024;
constexpr u32 WS_GATE = WS_EPOCH + 8192;
constexpr u32 LDS_TOTAL = 163840;
constexpr u32 QA_SLOT = 24576, QA0 = 65536, QB_SLOT = 16384;
constexpr u32 S2_REG = 35840;
constexpr u32 S2_BASE = 19456;
constexpr u32 L_HALO = 0;
constexpr u32 L_CNT = S2_BASE + 4 * S2_REG;

template <int N> DEVI void waitv() { asm volatile("s_waitcnt vmcnt(%0)" ::"n"(N) : "memory"); }
DEVI void waitl0() { asm volatile("s_waitcnt lgkmcnt(0)" ::: "memory"); }
DEVI void bar() { asm volatile("" ::: "memory"); __builtin_amdgcn_s_barrier(); asm volatile("" ::: "memory"); }
DEVI void dma1(const char* sb, u32 v0, u32 lds) {
  asm volatile("s_mov_b32 m0, %2\n\ts_nop 0\n\tglobal_load_lds_dwordx4 %0, %1" ::"v"(v0), "s"(sb), "s"(lds) : "memory");
}
DEVI void dma2(const char* sb, u32 v0, u32 v1, u32 lds) {
  asm volatile("s_mov_b32 m0, %3\n\ts_nop 0\n\tglobal_load_lds_dwordx4 %0, %2\n\tglobal_load_lds_dwordx4 %1, %2 offset:1024"
               ::"v"(v0), "v"(v1), "s"(sb), "s"(lds) : "memory");
}
DEVI void dma3(const char* sb, u32 v0, u32 v1, u32 v2, u32 lds) {
  asm volatile("s_mov_b32 m0, %4\n\ts_nop 0\n\tglobal_load_lds_dwordx4 %0, %3\n\tglobal_load_lds_dwordx4 %1, %3 offset:1024\n\t"
               "global_load_lds_dwordx4 %2, %3 offset:2048" ::"v"(v0), "v"(v1), "v"(v2), "s"(sb), "s"(lds) : "memory");
}
DEVI void dma4(const char* sb, u32 v0, u32 v1, u32 v2, u32 v3, u32 lds) {
  asm volatile("s_mov_b32 m0, %5\n\ts_nop 0\n\tglobal_load_lds_dwordx4 %0, %4\n\tglobal_load_lds_dwordx4 %1, %4 offset:1024\n\t"
               "global_load_lds_dwordx4 %2, %4 offset:2048\n\tglobal_load_lds_dwordx4 %3, %4 offset:3072"
               ::"v"(v0), "v"(v1), "v"(v2), "v"(v3), "s"(sb), "s"(lds) : "memory");
}
DEVI void touch(const char* sb, u32 v0, u32 lds) {
  asm volatile("s_mov_b32 m0, %2\n\ts_nop 0\n\tglobal_load_lds_dword %0, %1" ::"v"(v0), "s"(sb), "s"(lds) : "memory");
}
DEVI LDS3 char* lp(u32 a) { return (LDS3 char*)(size_t)a; }
DEVI bf16x8 lds128(u32 a) { return *(LDS3 bf16x8*)lp(a); }
DEVI bf16x4 lds64(u32 a) { return *(LDS3 bf16x4*)lp(a); }
DEVI bf16x4 ldstr(u32 a) { return __builtin_amdgcn_ds_read_tr16_b64_v4bf16((LDS3 bf16x4*)lp(a)); }
DEVI bf16x8 cat(bf16x4 a, bf16x4 b) { return __builtin_shufflevector(a, b, 0, 1, 2, 3, 4, 5, 6, 7); }
DEVI f32x4 mfma(bf16x8 a, bf16x8 b, f32x4 c) { return __builtin_amdgcn_mfma_f32_16x16x32_bf16(a, b, c, 0, 0, 0); }
DEVI bf16x8 b8(u32x4 v) { return __builtin_bit_cast(bf16x8, v); }
DEVI u32 pk2(float a, float b) { f32x2 v = {a, b}; return __builtin_bit_cast(u32, __builtin_convertvector(v, bf16x2)); }
DEVI float fmax2(float a, float b) { float r; asm("v_max_f32 %0, %1, %2" : "=v"(r) : "v"(a), "v"(b)); return r; }
DEVI float fmax2n(float a, float b) { float r; asm("v_max_f32 %0, %1, %2\n\ts_nop 1" : "=v"(r) : "v"(a), "v"(b)); return r; }
DEVI float fmax3n(float a, float b, float c) { float r; asm("v_max3_f32 %0, %1, %2, %3\n\ts_nop 1" : "=v"(r) : "v"(a), "v"(b), "v"(c)); return r; }
DEVI f32x2 sw16(float a, float b) { auto r = __builtin_amdgcn_permlane16_swap(__float_as_uint(a), __float_as_uint(b), false, false); return f32x2{__uint_as_float(r[0]), __uint_as_float(r[1])}; }
DEVI f32x2 sw32(float a, float b) { auto r = __builtin_amdgcn_permlane32_swap(__float_as_uint(a), __float_as_uint(b), false, false); return f32x2{__uint_as_float(r[0]), __uint_as_float(r[1])}; }
DEVI u64 ldu64(const char* p) { return __hip_atomic_load((GLB1 const u64*)p, __ATOMIC_RELAXED, __HIP_MEMORY_SCOPE_AGENT); }
DEVI void stu64(char* p, u64 v) { __hip_atomic_store((GLB1 u64*)p, v, __ATOMIC_RELAXED, __HIP_MEMORY_SCOPE_AGENT); }
DEVI u64 read_tag(const char* ws) {
  u64 t;
  asm volatile("s_load_dwordx2 %0, %1, %2\n\ts_waitcnt lgkmcnt(0)" : "=s"(t) : "s"(ws), "s"(WS_EPOCH) : "memory");
  return t + 1;
}
constexpr int SPIN_CAP = 1 << 20;
DEVI void poll(const char* p, bool active, u64 tag) {
#pragma nounroll
  for (int i = 0; i < SPIN_CAP; i++) {
    const u64 v = ldu64(p);
    if (__all(!active || v == tag)) break;
    __builtin_amdgcn_s_sleep(1);
  }
  asm volatile("" ::: "memory");
}
DEVI void publish(char* flag, u64 tag, int lane) {
  waitv<0>();
  if (lane == 0) {
    const u32 n = __hip_atomic_fetch_add((LDS3 u32*)lp(L_CNT), 1u, __ATOMIC_RELAXED, __HIP_MEMORY_SCOPE_WORKGROUP);
    if (n == 3) stu64(flag, tag);
  }
}

#define DFT_LOOP \
  "v_mov_b32 v16, 0\n\t" \
  "v_mov_b32 v17, 0\n\t" \
  "v_mov_b32 v18, 0\n\t" \
  "v_mov_b32 v19, 0\n\t" \
  "v_mov_b32 v20, 0\n\t" \
  "v_mov_b32 v21, 0\n\t" \
  "v_mov_b32 v22, 0\n\t" \
  "v_mov_b32 v23, 0\n\t" \
  "v_mov_b32 v24, 0\n\t" \
  "v_mov_b32 v25, 0\n\t" \
  "v_mov_b32 v26, 0\n\t" \
  "v_mov_b32 v27, 0\n\t" \
  "v_mov_b32 v28, 0\n\t" \
  "v_mov_b32 v29, 0\n\t" \
  "v_mov_b32 v30, 0\n\t" \
  "v_mov_b32 v31, 0\n\t" \
  "v_mov_b32 v32, 0\n\t" \
  "v_mov_b32 v33, 0\n\t" \
  "v_mov_b32 v34, 0\n\t" \
  "v_mov_b32 v35, 0\n\t" \
  "v_mov_b32 v36, 0\n\t" \
  "v_mov_b32 v37, 0\n\t" \
  "v_mov_b32 v38, 0\n\t" \
  "v_mov_b32 v39, 0\n\t" \
  "v_mov_b32 v40, 0\n\t" \
  "v_mov_b32 v41, 0\n\t" \
  "v_mov_b32 v42, 0\n\t" \
  "v_mov_b32 v43, 0\n\t" \
  "v_mov_b32 v44, 0\n\t" \
  "v_mov_b32 v45, 0\n\t" \
  "v_mov_b32 v46, 0\n\t" \
  "v_mov_b32 v47, 0\n\t" \
  "v_mov_b32 v48, 0\n\t" \
  "v_mov_b32 v49, 0\n\t" \
  "v_mov_b32 v50, 0\n\t" \
  "v_mov_b32 v51, 0\n\t" \
  "v_mov_b32 v52, 0\n\t" \
  "v_mov_b32 v53, 0\n\t" \
  "v_mov_b32 v54, 0\n\t" \
  "v_mov_b32 v55, 0\n\t" \
  "v_mov_b32 v56, 0\n\t" \
  "v_mov_b32 v57, 0\n\t" \
  "v_mov_b32 v58, 0\n\t" \
  "v_mov_b32 v59, 0\n\t" \
  "v_mov_b32 v60, 0\n\t" \
  "v_mov_b32 v61, 0\n\t" \
  "v_mov_b32 v62, 0\n\t" \
  "v_mov_b32 v63, 0\n\t" \
  "v_mov_b32 v64, 0\n\t" \
  "v_mov_b32 v65, 0\n\t" \
  "v_mov_b32 v66, 0\n\t" \
  "v_mov_b32 v67, 0\n\t" \
  "v_mov_b32 v68, 0\n\t" \
  "v_mov_b32 v69, 0\n\t" \
  "v_mov_b32 v70, 0\n\t" \
  "v_mov_b32 v71, 0\n\t" \
  "v_mov_b32 v72, 0\n\t" \
  "v_mov_b32 v73, 0\n\t" \
  "v_mov_b32 v74, 0\n\t" \
  "v_mov_b32 v75, 0\n\t" \
  "v_mov_b32 v76, 0\n\t" \
  "v_mov_b32 v77, 0\n\t" \
  "v_mov_b32 v78, 0\n\t" \
  "v_mov_b32 v79, 0\n\t" \
  "s_barrier\n\t" \
  "ds_read_b64_tr_b16 v[80:81], v10 offset:0\n\t" \
  "ds_read_b64_tr_b16 v[82:83], v10 offset:1024\n\t" \
  "ds_read_b128 v[96:99], v8 offset:0\n\t" \
  "ds_read_b128 v[100:103], v8 offset:2048\n\t" \
  "ds_read_b128 v[104:107], v8 offset:4096\n\t" \
  "ds_read_b128 v[108:111], v8 offset:6144\n\t" \
  "ds_read_b64_tr_b16 v[84:85], v11 offset:0\n\t" \
  "ds_read_b64_tr_b16 v[86:87], v11 offset:1024\n\t" \
  "ds_read_b64_tr_b16 v[88:89], v12 offset:0\n\t" \
  "ds_read_b64_tr_b16 v[90:91], v12 offset:1024\n\t" \
  "ds_read_b64_tr_b16 v[92:93], v13 offset:0\n\t" \
  "ds_read_b64_tr_b16 v[94:95], v13 offset:1024\n\t" \
  "Lt%=:\n\t" \
  "s_waitcnt lgkmcnt(0)\n\t" \
  "v_mfma_f32_16x16x32_bf16 v[16:19], v[80:83], v[96:99], v[16:19]\n\t" \
  "ds_read_b64_tr_b16 v[112:113], v10 offset:8192\n\t" \
  "ds_read_b64_tr_b16 v[114:115], v10 offset:9216\n\t" \
  "v_mfma_f32_16x16x32_bf16 v[20:23], v[80:83], v[100:103], v[20:23]\n\t" \
  "ds_read_b128 v[128:131], v9 offset:0\n\t" \
  "v_mfma_f32_16x16x32_bf16 v[24:27], v[80:83], v[104:107], v[24:27]\n\t" \
  "ds_read_b128 v[132:135], v9 offset:2048\n\t" \
  "ds_read_b128 v[136:139], v9 offset:4096\n\t" \
  "v_mfma_f32_16x16x32_bf16 v[28:31], v[80:83], v[108:111], v[28:31]\n\t" \
  "ds_read_b128 v[140:143], v9 offset:6144\n\t" \
  "v_mfma_f32_16x16x32_bf16 v[32:35], v[84:87], v[96:99], v[32:35]\n\t" \
  "ds_read_b64_tr_b16 v[116:117], v11 offset:8192\n\t" \
  "ds_read_b64_tr_b16 v[118:119], v11 offset:9216\n\t" \
  "v_mfma_f32_16x16x32_bf16 v[36:39], v[84:87], v[100:103], v[36:39]\n\t" \
  "ds_read_b64_tr_b16 v[120:121], v12 offset:8192\n\t" \
  "v_mfma_f32_16x16x32_bf16 v[40:43], v[84:87], v[104:107], v[40:43]\n\t" \
  "ds_read_b64_tr_b16 v[122:123], v12 offset:9216\n\t" \
  "ds_read_b64_tr_b16 v[124:125], v13 offset:8192\n\t" \
  "v_mfma_f32_16x16x32_bf16 v[44:47], v[84:87], v[108:111], v[44:47]\n\t" \
  "ds_read_b64_tr_b16 v[126:127], v13 offset:9216\n\t" \
  "v_mfma_f32_16x16x32_bf16 v[48:51], v[88:91], v[96:99], v[48:51]\n\t" \
  "v_mfma_f32_16x16x32_bf16 v[52:55], v[88:91], v[100:103], v[52:55]\n\t" \
  "v_mfma_f32_16x16x32_bf16 v[56:59], v[88:91], v[104:107], v[56:59]\n\t" \
  "v_mfma_f32_16x16x32_bf16 v[60:63], v[88:91], v[108:111], v[60:63]\n\t" \
  "v_mfma_f32_16x16x32_bf16 v[64:67], v[92:95], v[96:99], v[64:67]\n\t" \
  "v_mfma_f32_16x16x32_bf16 v[68:71], v[92:95], v[100:103], v[68:71]\n\t" \
  "v_mfma_f32_16x16x32_bf16 v[72:75], v[92:95], v[104:107], v[72:75]\n\t" \
  "v_mfma_f32_16x16x32_bf16 v[76:79], v[92:95], v[108:111], v[76:79]\n\t" \
  "s_waitcnt lgkmcnt(0)\n\t" \
  "s_barrier\n\t" \
  "v_mfma_f32_16x16x32_bf16 v[16:19], v[112:115], v[128:131], v[16:19]\n\t" \
  "ds_read_b64_tr_b16 v[80:81], v10 offset:16384\n\t" \
  "ds_read_b64_tr_b16 v[82:83], v10 offset:17408\n\t" \
  "v_mfma_f32_16x16x32_bf16 v[20:23], v[112:115], v[132:135], v[20:23]\n\t" \
  "ds_read_b128 v[96:99], v8 offset:16384\n\t" \
  "v_mfma_f32_16x16x32_bf16 v[24:27], v[112:115], v[136:139], v[24:27]\n\t" \
  "ds_read_b128 v[100:103], v8 offset:18432\n\t" \
  "ds_read_b128 v[104:107], v8 offset:20480\n\t" \
  "v_mfma_f32_16x16x32_bf16 v[28:31], v[112:115], v[140:143], v[28:31]\n\t" \
  "ds_read_b128 v[108:111], v8 offset:22528\n\t" \
  "v_mfma_f32_16x16x32_bf16 v[32:35], v[116:119], v[128:131], v[32:35]\n\t" \
  "ds_read_b64_tr_b16 v[84:85], v11 offset:16384\n\t" \
  "ds_read_b64_tr_b16 v[86:87], v11 offset:17408\n\t" \
  "v_mfma_f32_16x16x32_bf16 v[36:39], v[116:119], v[132:135], v[36:39]\n\t" \
  "ds_read_b64_tr_b16 v[88:89], v12 offset:16384\n\t" \
  "v_mfma_f32_16x16x32_bf16 v[40:43], v[116:119], v[136:139], v[40:43]\n\t" \
  "ds_read_b64_tr_b16 v[90:91], v12 offset:17408\n\t" \
  "ds_read_b64_tr_b16 v[92:93], v13 offset:16384\n\t" \
  "v_mfma_f32_16x16x32_bf16 v[44:47], v[116:119], v[140:143], v[44:47]\n\t" \
  "ds_read_b64_tr_b16 v[94:95], v13 offset:17408\n\t" \
  "v_mfma_f32_16x16x32_bf16 v[48:51], v[120:123], v[128:131], v[48:51]\n\t" \
  "v_mfma_f32_16x16x32_bf16 v[52:55], v[120:123], v[132:135], v[52:55]\n\t" \
  "v_mfma_f32_16x16x32_bf16 v[56:59], v[120:123], v[136:139], v[56:59]\n\t" \
  "v_mfma_f32_16x16x32_bf16 v[60:63], v[120:123], v[140:143], v[60:63]\n\t" \
  "v_mfma_f32_16x16x32_bf16 v[64:67], v[124:127], v[128:131], v[64:67]\n\t" \
  "v_mfma_f32_16x16x32_bf16 v[68:71], v[124:127], v[132:135], v[68:71]\n\t" \
  "v_mfma_f32_16x16x32_bf16 v[72:75], v[124:127], v[136:139], v[72:75]\n\t" \
  "v_mfma_f32_16x16x32_bf16 v[76:79], v[124:127], v[140:143], v[76:79]\n\t" \
  "s_waitcnt lgkmcnt(0)\n\t" \
  "v_mfma_f32_16x16x32_bf16 v[16:19], v[80:83], v[96:99], v[16:19]\n\t" \
  "ds_read_b64_tr_b16 v[112:113], v10 offset:24576\n\t" \
  "ds_read_b64_tr_b16 v[114:115], v10 offset:25600\n\t" \
  "v_mfma_f32_16x16x32_bf16 v[20:23], v[80:83], v[100:103], v[20:23]\n\t" \
  "ds_read_b128 v[128:131], v9 offset:16384\n\t" \
  "v_mfma_f32_16x16x32_bf16 v[24:27], v[80:83], v[104:107], v[24:27]\n\t" \
  "ds_read_b128 v[132:135], v9 offset:18432\n\t" \
  "ds_read_b128 v[136:139], v9 offset:20480\n\t" \
  "v_mfma_f32_16x16x32_bf16 v[28:31], v[80:83], v[108:111], v[28:31]\n\t" \
  "ds_read_b128 v[140:143], v9 offset:22528\n\t" \
  "v_mfma_f32_16x16x32_bf16 v[32:35], v[84:87], v[96:99], v[32:35]\n\t" \
  "ds_read_b64_tr_b16 v[116:117], v11 offset:24576\n\t" \
  "ds_read_b64_tr_b16 v[118:119], v11 offset:25600\n\t" \
  "v_mfma_f32_16x16x32_bf16 v[36:39], v[84:87], v[100:103], v[36:39]\n\t" \
  "ds_read_b64_tr_b16 v[120:121], v12 offset:24576\n\t" \
  "v_mfma_f32_16x16x32_bf16 v[40:43], v[84:87], v[104:107], v[40:43]\n\t" \
  "ds_read_b64_tr_b16 v[122:123], v12 offset:25600\n\t" \
  "ds_read_b64_tr_b16 v[124:125], v13 offset:24576\n\t" \
  "v_mfma_f32_16x16x32_bf16 v[44:47], v[84:87], v[108:111], v[44:47]\n\t" \
  "ds_read_b64_tr_b16 v[126:127], v13 offset:25600\n\t" \
  "v_mfma_f32_16x16x32_bf16 v[48:51], v[88:91], v[96:99], v[48:51]\n\t" \
  "v_xor_b32 v8, 0x8000, v8\n\t" \
  "v_mfma_f32_16x16x32_bf16 v[52:55], v[88:91], v[100:103], v[52:55]\n\t" \
  "v_xor_b32 v9, 0x8000, v9\n\t" \
  "v_mfma_f32_16x16x32_bf16 v[56:59], v[88:91], v[104:107], v[56:59]\n\t" \
  "v_xor_b32 v10, 0x8000, v10\n\t" \
  "v_mfma_f32_16x16x32_bf16 v[60:63], v[88:91], v[108:111], v[60:63]\n\t" \
  "v_xor_b32 v11, 0x8000, v11\n\t" \
  "v_mfma_f32_16x16x32_bf16 v[64:67], v[92:95], v[96:99], v[64:67]\n\t" \
  "v_xor_b32 v12, 0x8000, v12\n\t" \
  "v_mfma_f32_16x16x32_bf16 v[68:71], v[92:95], v[100:103], v[68:71]\n\t" \
  "v_xor_b32 v13, 0x8000, v13\n\t" \
  "v_mfma_f32_16x16x32_bf16 v[72:75], v[92:95], v[104:107], v[72:75]\n\t" \
  "v_mfma_f32_16x16x32_bf16 v[76:79], v[92:95], v[108:111], v[76:79]\n\t" \
  "s_sub_u32 %[cnt], %[cnt], 1\n\t" \
  "s_cmp_eq_u32 %[cnt], 0\n\t" \
  "s_waitcnt lgkmcnt(0)\n\t" \
  "s_barrier\n\t" \
  "s_cbranch_scc1 Le%=\n\t" \
  "v_mfma_f32_16x16x32_bf16 v[16:19], v[112:115], v[128:131], v[16:19]\n\t" \
  "ds_read_b64_tr_b16 v[80:81], v10 offset:0\n\t" \
  "ds_read_b64_tr_b16 v[82:83], v10 offset:1024\n\t" \
  "v_mfma_f32_16x16x32_bf16 v[20:23], v[112:115], v[132:135], v[20:23]\n\t" \
  "ds_read_b128 v[96:99], v8 offset:0\n\t" \
  "v_mfma_f32_16x16x32_bf16 v[24:27], v[112:115], v[136:139], v[24:27]\n\t" \
  "ds_read_b128 v[100:103], v8 offset:2048\n\t" \
  "ds_read_b128 v[104:107], v8 offset:4096\n\t" \
  "v_mfma_f32_16x16x32_bf16 v[28:31], v[112:115], v[140:143], v[28:31]\n\t" \
  "ds_read_b128 v[108:111], v8 offset:6144\n\t" \
  "v_mfma_f32_16x16x32_bf16 v[32:35], v[116:119], v[128:131], v[32:35]\n\t" \
  "ds_read_b64_tr_b16 v[84:85], v11 offset:0\n\t" \
  "ds_read_b64_tr_b16 v[86:87], v11 offset:1024\n\t" \
  "v_mfma_f32_16x16x32_bf16 v[36:39], v[116:119], v[132:135], v[36:39]\n\t" \
  "ds_read_b64_tr_b16 v[88:89], v12 offset:0\n\t" \
  "v_mfma_f32_16x16x32_bf16 v[40:43], v[116:119], v[136:139], v[40:43]\n\t" \
  "ds_read_b64_tr_b16 v[90:91], v12 offset:1024\n\t" \
  "ds_read_b64_tr_b16 v[92:93], v13 offset:0\n\t" \
  "v_mfma_f32_16x16x32_bf16 v[44:47], v[116:119], v[140:143], v[44:47]\n\t" \
  "ds_read_b64_tr_b16 v[94:95], v13 offset:1024\n\t" \
  "v_mfma_f32_16x16x32_bf16 v[48:51], v[120:123], v[128:131], v[48:51]\n\t" \
  "v_mfma_f32_16x16x32_bf16 v[52:55], v[120:123], v[132:135], v[52:55]\n\t" \
  "v_mfma_f32_16x16x32_bf16 v[56:59], v[120:123], v[136:139], v[56:59]\n\t" \
  "v_mfma_f32_16x16x32_bf16 v[60:63], v[120:123], v[140:143], v[60:63]\n\t" \
  "v_mfma_f32_16x16x32_bf16 v[64:67], v[124:127], v[128:131], v[64:67]\n\t" \
  "v_mfma_f32_16x16x32_bf16 v[68:71], v[124:127], v[132:135], v[68:71]\n\t" \
  "v_mfma_f32_16x16x32_bf16 v[72:75], v[124:127], v[136:139], v[72:75]\n\t" \
  "v_mfma_f32_16x16x32_bf16 v[76:79], v[124:127], v[140:143], v[76:79]\n\t" \
  "s_branch Lt%=\n\t" \
  "Le%=:\n\t" \
  "v_mfma_f32_16x16x32_bf16 v[16:19], v[112:115], v[128:131], v[16:19]\n\t" \
  "v_mfma_f32_16x16x32_bf16 v[32:35], v[116:119], v[128:131], v[32:35]\n\t" \
  "v_mfma_f32_16x16x32_bf16 v[48:51], v[120:123], v[128:131], v[48:51]\n\t" \
  "v_mfma_f32_16x16x32_bf16 v[64:67], v[124:127], v[128:131], v[64:67]\n\t" \
  "v_mfma_f32_16x16x32_bf16 v[20:23], v[112:115], v[132:135], v[20:23]\n\t" \
  "v_mfma_f32_16x16x32_bf16 v[36:39], v[116:119], v[132:135], v[36:39]\n\t" \
  "v_mfma_f32_16x16x32_bf16 v[52:55], v[120:123], v[132:135], v[52:55]\n\t" \
  "v_mfma_f32_16x16x32_bf16 v[68:71], v[124:127], v[132:135], v[68:71]\n\t" \
  "v_mfma_f32_16x16x32_bf16 v[24:27], v[112:115], v[136:139], v[24:27]\n\t" \
  "v_mfma_f32_16x16x32_bf16 v[40:43], v[116:119], v[136:139], v[40:43]\n\t" \
  "v_mfma_f32_16x16x32_bf16 v[56:59], v[120:123], v[136:139], v[56:59]\n\t" \
  "v_mfma_f32_16x16x32_bf16 v[72:75], v[124:127], v[136:139], v[72:75]\n\t" \
  "s_nop 0\n\t" \
  "v_cvt_pk_bf16_f32 v80, v16, v17\n\t" \
  "v_cvt_pk_bf16_f32 v81, v18, v19\n\t" \
  "v_cvt_pk_bf16_f32 v82, v32, v33\n\t" \
  "v_mfma_f32_16x16x32_bf16 v[28:31], v[112:115], v[140:143], v[28:31]\n\t" \
  "v_cvt_pk_bf16_f32 v83, v34, v35\n\t" \
  "global_store_dwordx4 v144, v[80:83], %[ab] offset:0 sc1\n\t" \
  "v_cvt_pk_bf16_f32 v84, v48, v49\n\t" \
  "v_mfma_f32_16x16x32_bf16 v[44:47], v[116:119], v[140:143], v[44:47]\n\t" \
  "v_cvt_pk_bf16_f32 v85, v50, v51\n\t" \
  "v_cvt_pk_bf16_f32 v86, v64, v65\n\t" \
  "v_cvt_pk_bf16_f32 v87, v66, v67\n\t" \
  "v_mfma_f32_16x16x32_bf16 v[60:63], v[120:123], v[140:143], v[60:63]\n\t" \
  "global_store_dwordx4 v144, v[84:87], %[ab] offset:1024 sc1\n\t" \
  "v_mfma_f32_16x16x32_bf16 v[76:79], v[124:127], v[140:143], v[76:79]\n\t" \
  "v_cvt_pk_bf16_f32 v88, v20, v21\n\t" \
  "v_cvt_pk_bf16_f32 v89, v22, v23\n\t" \
  "v_cvt_pk_bf16_f32 v90, v36, v37\n\t" \
  "v_cvt_pk_bf16_f32 v91, v38, v39\n\t" \
  "global_store_dwordx4 v145, v[88:91], %[ab] offset:0 sc1\n\t" \
  "v_cvt_pk_bf16_f32 v92, v52, v53\n\t" \
  "v_cvt_pk_bf16_f32 v93, v54, v55\n\t" \
  "v_cvt_pk_bf16_f32 v94, v68, v69\n\t" \
  "v_cvt_pk_bf16_f32 v95, v70, v71\n\t" \
  "global_store_dwordx4 v145, v[92:95], %[ab] offset:1024 sc1\n\t" \
  "v_cvt_pk_bf16_f32 v96, v24, v25\n\t" \
  "v_cvt_pk_bf16_f32 v97, v26, v27\n\t" \
  "v_cvt_pk_bf16_f32 v98, v40, v41\n\t" \
  "v_cvt_pk_bf16_f32 v99, v42, v43\n\t" \
  "global_store_dwordx4 v146, v[96:99], %[ab] offset:0 sc1\n\t" \
  "v_cvt_pk_bf16_f32 v100, v56, v57\n\t" \
  "v_cvt_pk_bf16_f32 v101, v58, v59\n\t" \
  "v_cvt_pk_bf16_f32 v102, v72, v73\n\t" \
  "v_cvt_pk_bf16_f32 v103, v74, v75\n\t" \
  "global_store_dwordx4 v146, v[100:103], %[ab] offset:1024 sc1\n\t" \
  "v_cvt_pk_bf16_f32 v104, v28, v29\n\t" \
  "v_cvt_pk_bf16_f32 v105, v30, v31\n\t" \
  "v_cvt_pk_bf16_f32 v106, v44, v45\n\t" \
  "v_cvt_pk_bf16_f32 v107, v46, v47\n\t" \
  "global_store_dwordx4 v147, v[104:107], %[ab] offset:0 sc1\n\t" \
  "v_cvt_pk_bf16_f32 v108, v60, v61\n\t" \
  "v_cvt_pk_bf16_f32 v109, v62, v63\n\t" \
  "v_cvt_pk_bf16_f32 v110, v76, v77\n\t" \
  "v_cvt_pk_bf16_f32 v111, v78, v79\n\t" \
  "global_store_dwordx4 v147, v[108:111], %[ab] offset:1024 sc1\n\t" \
  "s_barrier\n\t" \
  ""
#define QKV_LOOP \
  "v_mov_b32 v66, 0\n\t" \
  "v_mov_b32 v67, 0\n\t" \
  "v_mov_b32 v68, 0\n\t" \
  "v_mov_b32 v69, 0\n\t" \
  "v_mov_b32 v70, 0\n\t" \
  "v_mov_b32 v71, 0\n\t" \
  "v_mov_b32 v72, 0\n\t" \
  "v_mov_b32 v73, 0\n\t" \
  "v_mov_b32 v74, 0\n\t" \
  "v_mov_b32 v75, 0\n\t" \
  "v_mov_b32 v76, 0\n\t" \
  "v_mov_b32 v77, 0\n\t" \
  "v_mov_b32 v78, 0\n\t" \
  "v_mov_b32 v79, 0\n\t" \
  "v_mov_b32 v80, 0\n\t" \
  "v_mov_b32 v81, 0\n\t" \
  "v_mov_b32 v82, 0\n\t" \
  "v_mov_b32 v83, 0\n\t" \
  "v_mov_b32 v84, 0\n\t" \
  "v_mov_b32 v85, 0\n\t" \
  "v_mov_b32 v86, 0\n\t" \
  "v_mov_b32 v87, 0\n\t" \
  "v_mov_b32 v88, 0\n\t" \
  "v_mov_b32 v89, 0\n\t" \
  "v_mov_b32 v90, 0\n\t" \
  "v_mov_b32 v91, 0\n\t" \
  "v_mov_b32 v92, 0\n\t" \
  "v_mov_b32 v93, 0\n\t" \
  "v_mov_b32 v94, 0\n\t" \
  "v_mov_b32 v95, 0\n\t" \
  "v_mov_b32 v96, 0\n\t" \
  "v_mov_b32 v97, 0\n\t" \
  "v_mov_b32 v98, 0\n\t" \
  "v_mov_b32 v99, 0\n\t" \
  "v_mov_b32 v100, 0\n\t" \
  "v_mov_b32 v101, 0\n\t" \
  "v_mov_b32 v102, 0\n\t" \
  "v_mov_b32 v103, 0\n\t" \
  "v_mov_b32 v104, 0\n\t" \
  "v_mov_b32 v105, 0\n\t" \
  "v_mov_b32 v106, 0\n\t" \
  "v_mov_b32 v107, 0\n\t" \
  "v_mov_b32 v108, 0\n\t" \
  "v_mov_b32 v109, 0\n\t" \
  "v_mov_b32 v110, 0\n\t" \
  "v_mov_b32 v111, 0\n\t" \
  "v_mov_b32 v112, 0\n\t" \
  "v_mov_b32 v113, 0\n\t" \
  "v_mov_b32 v114, 0\n\t" \
  "v_mov_b32 v115, 0\n\t" \
  "v_mov_b32 v116, 0\n\t" \
  "v_mov_b32 v117, 0\n\t" \
  "v_mov_b32 v118, 0\n\t" \
  "v_mov_b32 v119, 0\n\t" \
  "v_mov_b32 v120, 0\n\t" \
  "v_mov_b32 v121, 0\n\t" \
  "v_mov_b32 v122, 0\n\t" \
  "v_mov_b32 v123, 0\n\t" \
  "v_mov_b32 v124, 0\n\t" \
  "v_mov_b32 v125, 0\n\t" \
  "v_mov_b32 v126, 0\n\t" \
  "v_mov_b32 v127, 0\n\t" \
  "v_mov_b32 v128, 0\n\t" \
  "v_mov_b32 v129, 0\n\t" \
  "v_mov_b32 v130, 0\n\t" \
  "v_mov_b32 v131, 0\n\t" \
  "v_mov_b32 v132, 0\n\t" \
  "v_mov_b32 v133, 0\n\t" \
  "v_mov_b32 v134, 0\n\t" \
  "v_mov_b32 v135, 0\n\t" \
  "v_mov_b32 v136, 0\n\t" \
  "v_mov_b32 v137, 0\n\t" \
  "v_mov_b32 v138, 0\n\t" \
  "v_mov_b32 v139, 0\n\t" \
  "v_mov_b32 v140, 0\n\t" \
  "v_mov_b32 v141, 0\n\t" \
  "v_mov_b32 v142, 0\n\t" \
  "v_mov_b32 v143, 0\n\t" \
  "v_mov_b32 v144, 0\n\t" \
  "v_mov_b32 v145, 0\n\t" \
  "v_mov_b32 v146, 0\n\t" \
  "v_mov_b32 v147, 0\n\t" \
  "v_mov_b32 v148, 0\n\t" \
  "v_mov_b32 v149, 0\n\t" \
  "v_mov_b32 v150, 0\n\t" \
  "v_mov_b32 v151, 0\n\t" \
  "v_mov_b32 v152, 0\n\t" \
  "v_mov_b32 v153, 0\n\t" \
  "v_mov_b32 v154, 0\n\t" \
  "v_mov_b32 v155, 0\n\t" \
  "v_mov_b32 v156, 0\n\t" \
  "v_mov_b32 v157, 0\n\t" \
  "v_mov_b32 v158, 0\n\t" \
  "v_mov_b32 v159, 0\n\t" \
  "v_mov_b32 v160, 0\n\t" \
  "v_mov_b32 v161, 0\n\t" \
  "v_mov_b32 v162, 0\n\t" \
  "v_mov_b32 v163, 0\n\t" \
  "v_mov_b32 v164, 0\n\t" \
  "v_mov_b32 v165, 0\n\t" \
  "v_mov_b32 v166, 0\n\t" \
  "v_mov_b32 v167, 0\n\t" \
  "v_mov_b32 v168, 0\n\t" \
  "v_mov_b32 v169, 0\n\t" \
  "v_mov_b32 v170, 0\n\t" \
  "v_mov_b32 v171, 0\n\t" \
  "v_mov_b32 v172, 0\n\t" \
  "v_mov_b32 v173, 0\n\t" \
  "v_mov_b32 v174, 0\n\t" \
  "v_mov_b32 v175, 0\n\t" \
  "v_mov_b32 v176, 0\n\t" \
  "v_mov_b32 v177, 0\n\t" \
  "v_mov_b32 v178, 0\n\t" \
  "v_mov_b32 v179, 0\n\t" \
  "v_mov_b32 v180, 0\n\t" \
  "v_mov_b32 v181, 0\n\t" \
  "v_mov_b32 v182, 0\n\t" \
  "v_mov_b32 v183, 0\n\t" \
  "v_mov_b32 v184, 0\n\t" \
  "v_mov_b32 v185, 0\n\t" \
  "v_mov_b32 v186, 0\n\t" \
  "v_mov_b32 v187, 0\n\t" \
  "v_mov_b32 v188, 0\n\t" \
  "v_mov_b32 v189, 0\n\t" \
  "v_mov_b32 v190, 0\n\t" \
  "v_mov_b32 v191, 0\n\t" \
  "v_mov_b32 v192, 0\n\t" \
  "v_mov_b32 v193, 0\n\t" \
  "v_mov_b32 v194, 0\n\t" \
  "v_mov_b32 v195, 0\n\t" \
  "v_mov_b32 v196, 0\n\t" \
  "v_mov_b32 v197, 0\n\t" \
  "v_mov_b32 v198, 0\n\t" \
  "v_mov_b32 v199, 0\n\t" \
  "v_mov_b32 v200, 0\n\t" \
  "v_mov_b32 v201, 0\n\t" \
  "v_mov_b32 v202, 0\n\t" \
  "v_mov_b32 v203, 0\n\t" \
  "v_mov_b32 v204, 0\n\t" \
  "v_mov_b32 v205, 0\n\t" \
  "v_mov_b32 v206, 0\n\t" \
  "v_mov_b32 v207, 0\n\t" \
  "v_mov_b32 v208, 0\n\t" \
  "v_mov_b32 v209, 0\n\t" \
  "v_mov_b32 v210, 0\n\t" \
  "v_mov_b32 v211, 0\n\t" \
  "v_mov_b32 v212, 0\n\t" \
  "v_mov_b32 v213, 0\n\t" \
  "v_mov_b32 v214, 0\n\t" \
  "v_mov_b32 v215, 0\n\t" \
  "v_mov_b32 v216, 0\n\t" \
  "v_mov_b32 v217, 0\n\t" \
  "v_mov_b32 v218, 0\n\t" \
  "v_mov_b32 v219, 0\n\t" \
  "v_mov_b32 v220, 0\n\t" \
  "v_mov_b32 v221, 0\n\t" \
  "v_mov_b32 v222, 0\n\t" \
  "v_mov_b32 v223, 0\n\t" \
  "v_mov_b32 v224, 0\n\t" \
  "v_mov_b32 v225, 0\n\t" \
  "v_mov_b32 v226, 0\n\t" \
  "v_mov_b32 v227, 0\n\t" \
  "v_mov_b32 v228, 0\n\t" \
  "v_mov_b32 v229, 0\n\t" \
  "v_mov_b32 v230, 0\n\t" \
  "v_mov_b32 v231, 0\n\t" \
  "v_mov_b32 v232, 0\n\t" \
  "v_mov_b32 v233, 0\n\t" \
  "v_mov_b32 v234, 0\n\t" \
  "v_mov_b32 v235, 0\n\t" \
  "v_mov_b32 v236, 0\n\t" \
  "v_mov_b32 v237, 0\n\t" \
  "v_mov_b32 v238, 0\n\t" \
  "v_mov_b32 v239, 0\n\t" \
  "v_mov_b32 v240, 0\n\t" \
  "v_mov_b32 v241, 0\n\t" \
  "v_mov_b32 v242, 0\n\t" \
  "v_mov_b32 v243, 0\n\t" \
  "v_mov_b32 v244, 0\n\t" \
  "v_mov_b32 v245, 0\n\t" \
  "s_barrier\n\t" \
  "ds_read_b128 v[22:25], v20 offset:0\n\t" \
  "ds_read_b64_tr_b16 v[46:47], v12 offset:0\n\t" \
  "ds_read_b64_tr_b16 v[48:49], v12 offset:2048\n\t" \
  "ds_read_b128 v[26:29], v20 offset:2048\n\t" \
  "ds_read_b128 v[30:33], v20 offset:4096\n\t" \
  "ds_read_b64_tr_b16 v[50:51], v13 offset:0\n\t" \
  "ds_read_b64_tr_b16 v[52:53], v13 offset:2048\n\t" \
  "ds_read_b64_tr_b16 v[54:55], v14 offset:0\n\t" \
  "ds_read_b64_tr_b16 v[56:57], v14 offset:2048\n\t" \
  "ds_read_b64_tr_b16 v[58:59], v15 offset:0\n\t" \
  "ds_read_b64_tr_b16 v[60:61], v15 offset:2048\n\t" \
  "Lt%=:\n\t" \
  "s_waitcnt lgkmcnt(8)\n\t" \
  "v_mfma_f32_16x16x32_bf16 v[66:69], v[46:49], v[22:25], v[66:69]\n\t" \
  "ds_read_b64_tr_b16 v[62:63], v16 offset:0\n\t" \
  "s_waitcnt lgkmcnt(8)\n\t" \
  "v_mfma_f32_16x16x32_bf16 v[126:129], v[46:49], v[26:29], v[126:129]\n\t" \
  "ds_read_b64_tr_b16 v[64:65], v16 offset:2048\n\t" \
  "s_waitcnt lgkmcnt(7)\n\t" \
  "v_mfma_f32_16x16x32_bf16 v[186:189], v[46:49], v[30:33], v[186:189]\n\t" \
  "s_waitcnt lgkmcnt(6)\n\t" \
  "v_mfma_f32_16x16x32_bf16 v[70:73], v[50:53], v[22:25], v[70:73]\n\t" \
  "ds_read_b64_tr_b16 v[46:47], v17 offset:0\n\t" \
  "v_mfma_f32_16x16x32_bf16 v[130:133], v[50:53], v[26:29], v[130:133]\n\t" \
  "ds_read_b64_tr_b16 v[48:49], v17 offset:2048\n\t" \
  "v_mfma_f32_16x16x32_bf16 v[190:193], v[50:53], v[30:33], v[190:193]\n\t" \
  "s_waitcnt lgkmcnt(6)\n\t" \
  "v_mfma_f32_16x16x32_bf16 v[74:77], v[54:57], v[22:25], v[74:77]\n\t" \
  "ds_read_b64_tr_b16 v[50:51], v18 offset:0\n\t" \
  "v_mfma_f32_16x16x32_bf16 v[134:137], v[54:57], v[26:29], v[134:137]\n\t" \
  "ds_read_b64_tr_b16 v[52:53], v18 offset:2048\n\t" \
  "v_mfma_f32_16x16x32_bf16 v[194:197], v[54:57], v[30:33], v[194:197]\n\t" \
  "v_xor_b32 v20, 64, v20\n\t" \
  "s_waitcnt lgkmcnt(6)\n\t" \
  "v_mfma_f32_16x16x32_bf16 v[78:81], v[58:61], v[22:25], v[78:81]\n\t" \
  "ds_read_b64_tr_b16 v[54:55], v19 offset:0\n\t" \
  "v_mfma_f32_16x16x32_bf16 v[138:141], v[58:61], v[26:29], v[138:141]\n\t" \
  "ds_read_b64_tr_b16 v[56:57], v19 offset:2048\n\t" \
  "v_mfma_f32_16x16x32_bf16 v[198:201], v[58:61], v[30:33], v[198:201]\n\t" \
  "s_waitcnt lgkmcnt(6)\n\t" \
  "v_mfma_f32_16x16x32_bf16 v[82:85], v[62:65], v[22:25], v[82:85]\n\t" \
  "ds_read_b64_tr_b16 v[58:59], v12 offset:256\n\t" \
  "v_mfma_f32_16x16x32_bf16 v[142:145], v[62:65], v[26:29], v[142:145]\n\t" \
  "ds_read_b64_tr_b16 v[60:61], v12 offset:2304\n\t" \
  "v_mfma_f32_16x16x32_bf16 v[202:205], v[62:65], v[30:33], v[202:205]\n\t" \
  "s_waitcnt lgkmcnt(6)\n\t" \
  "v_mfma_f32_16x16x32_bf16 v[86:89], v[46:49], v[22:25], v[86:89]\n\t" \
  "ds_read_b64_tr_b16 v[62:63], v13 offset:256\n\t" \
  "v_mfma_f32_16x16x32_bf16 v[146:149], v[46:49], v[26:29], v[146:149]\n\t" \
  "ds_read_b64_tr_b16 v[64:65], v13 offset:2304\n\t" \
  "v_mfma_f32_16x16x32_bf16 v[206:209], v[46:49], v[30:33], v[206:209]\n\t" \
  "s_waitcnt lgkmcnt(6)\n\t" \
  "v_mfma_f32_16x16x32_bf16 v[90:93], v[50:53], v[22:25], v[90:93]\n\t" \
  "ds_read_b64_tr_b16 v[46:47], v14 offset:256\n\t" \
  "v_mfma_f32_16x16x32_bf16 v[150:153], v[50:53], v[26:29], v[150:153]\n\t" \
  "ds_read_b64_tr_b16 v[48:49], v14 offset:2304\n\t" \
  "v_mfma_f32_16x16x32_bf16 v[210:213], v[50:53], v[30:33], v[210:213]\n\t" \
  "s_waitcnt lgkmcnt(6)\n\t" \
  "v_mfma_f32_16x16x32_bf16 v[94:97], v[54:57], v[22:25], v[94:97]\n\t" \
  "ds_read_b64_tr_b16 v[50:51], v15 offset:256\n\t" \
  "v_mfma_f32_16x16x32_bf16 v[154:157], v[54:57], v[26:29], v[154:157]\n\t" \
  "ds_read_b64_tr_b16 v[52:53], v15 offset:2304\n\t" \
  "v_mfma_f32_16x16x32_bf16 v[214:217], v[54:57], v[30:33], v[214:217]\n\t" \
  "s_waitcnt lgkmcnt(6)\n\t" \
  "v_mfma_f32_16x16x32_bf16 v[98:101], v[58:61], v[22:25], v[98:101]\n\t" \
  "ds_read_b64_tr_b16 v[54:55], v16 offset:256\n\t" \
  "v_mfma_f32_16x16x32_bf16 v[158:161], v[58:61], v[26:29], v[158:161]\n\t" \
  "ds_read_b64_tr_b16 v[56:57], v16 offset:2304\n\t" \
  "v_mfma_f32_16x16x32_bf16 v[218:221], v[58:61], v[30:33], v[218:221]\n\t" \
  "s_waitcnt lgkmcnt(6)\n\t" \
  "v_mfma_f32_16x16x32_bf16 v[102:105], v[62:65], v[22:25], v[102:105]\n\t" \
  "ds_read_b64_tr_b16 v[58:59], v17 offset:256\n\t" \
  "v_mfma_f32_16x16x32_bf16 v[162:165], v[62:65], v[26:29], v[162:165]\n\t" \
  "ds_read_b64_tr_b16 v[60:61], v17 offset:2304\n\t" \
  "v_mfma_f32_16x16x32_bf16 v[222:225], v[62:65], v[30:33], v[222:225]\n\t" \
  "s_waitcnt lgkmcnt(6)\n\t" \
  "v_mfma_f32_16x16x32_bf16 v[106:109], v[22:25], v[46:49], v[106:109]\n\t" \
  "ds_read_b64_tr_b16 v[62:63], v18 offset:256\n\t" \
  "v_mfma_f32_16x16x32_bf16 v[166:169], v[26:29], v[46:49], v[166:169]\n\t" \
  "ds_read_b64_tr_b16 v[64:65], v18 offset:2304\n\t" \
  "v_mfma_f32_16x16x32_bf16 v[226:229], v[30:33], v[46:49], v[226:229]\n\t" \
  "s_barrier\n\t" \
  "s_waitcnt lgkmcnt(6)\n\t" \
  "v_mfma_f32_16x16x32_bf16 v[110:113], v[22:25], v[50:53], v[110:113]\n\t" \
  "ds_read_b64_tr_b16 v[46:47], v12 offset:16384\n\t" \
  "ds_read_b128 v[34:37], v20 offset:0\n\t" \
  "v_mfma_f32_16x16x32_bf16 v[170:173], v[26:29], v[50:53], v[170:173]\n\t" \
  "ds_read_b64_tr_b16 v[48:49], v12 offset:18432\n\t" \
  "ds_read_b128 v[38:41], v20 offset:2048\n\t" \
  "v_mfma_f32_16x16x32_bf16 v[230:233], v[30:33], v[50:53], v[230:233]\n\t" \
  "s_waitcnt lgkmcnt(8)\n\t" \
  "v_mfma_f32_16x16x32_bf16 v[114:117], v[22:25], v[54:57], v[114:117]\n\t" \
  "ds_read_b64_tr_b16 v[50:51], v13 offset:16384\n\t" \
  "ds_read_b128 v[42:45], v20 offset:4096\n\t" \
  "v_mfma_f32_16x16x32_bf16 v[174:177], v[26:29], v[54:57], v[174:177]\n\t" \
  "ds_read_b64_tr_b16 v[52:53], v13 offset:18432\n\t" \
  "v_mfma_f32_16x16x32_bf16 v[234:237], v[30:33], v[54:57], v[234:237]\n\t" \
  "s_waitcnt lgkmcnt(9)\n\t" \
  "v_mfma_f32_16x16x32_bf16 v[118:121], v[22:25], v[58:61], v[118:121]\n\t" \
  "ds_read_b64_tr_b16 v[54:55], v14 offset:16384\n\t" \
  "v_mfma_f32_16x16x32_bf16 v[178:181], v[26:29], v[58:61], v[178:181]\n\t" \
  "ds_read_b64_tr_b16 v[56:57], v14 offset:18432\n\t" \
  "v_mfma_f32_16x16x32_bf16 v[238:241], v[30:33], v[58:61], v[238:241]\n\t" \
  "s_waitcnt lgkmcnt(9)\n\t" \
  "v_mfma_f32_16x16x32_bf16 v[122:125], v[22:25], v[62:65], v[122:125]\n\t" \
  "ds_read_b64_tr_b16 v[58:59], v15 offset:16384\n\t" \
  "v_mfma_f32_16x16x32_bf16 v[182:185], v[26:29], v[62:65], v[182:185]\n\t" \
  "ds_read_b64_tr_b16 v[60:61], v15 offset:18432\n\t" \
  "v_mfma_f32_16x16x32_bf16 v[242:245], v[30:33], v[62:65], v[242:245]\n\t" \
  "s_waitcnt lgkmcnt(8)\n\t" \
  "v_mfma_f32_16x16x32_bf16 v[66:69], v[46:49], v[34:37], v[66:69]\n\t" \
  "ds_read_b64_tr_b16 v[62:63], v16 offset:16384\n\t" \
  "s_waitcnt lgkmcnt(8)\n\t" \
  "v_mfma_f32_16x16x32_bf16 v[126:129], v[46:49], v[38:41], v[126:129]\n\t" \
  "ds_read_b64_tr_b16 v[64:65], v16 offset:18432\n\t" \
  "s_waitcnt lgkmcnt(7)\n\t" \
  "v_mfma_f32_16x16x32_bf16 v[186:189], v[46:49], v[42:45], v[186:189]\n\t" \
  "s_waitcnt lgkmcnt(6)\n\t" \
  "v_mfma_f32_16x16x32_bf16 v[70:73], v[50:53], v[34:37], v[70:73]\n\t" \
  "ds_read_b64_tr_b16 v[46:47], v17 offset:16384\n\t" \
  "v_mfma_f32_16x16x32_bf16 v[130:133], v[50:53], v[38:41], v[130:133]\n\t" \
  "ds_read_b64_tr_b16 v[48:49], v17 offset:18432\n\t" \
  "v_mfma_f32_16x16x32_bf16 v[190:193], v[50:53], v[42:45], v[190:193]\n\t" \
  "s_add_u32 %[ao], %[ao], 0x6000\n\t" \
  "s_cmp_eq_u32 %[ao], 0x12000\n\t" \
  "s_waitcnt lgkmcnt(6)\n\t" \
  "v_mfma_f32_16x16x32_bf16 v[74:77], v[54:57], v[34:37], v[74:77]\n\t" \
  "ds_read_b64_tr_b16 v[50:51], v18 offset:16384\n\t" \
  "v_mfma_f32_16x16x32_bf16 v[134:137], v[54:57], v[38:41], v[134:137]\n\t" \
  "ds_read_b64_tr_b16 v[52:53], v18 offset:18432\n\t" \
  "v_mfma_f32_16x16x32_bf16 v[194:197], v[54:57], v[42:45], v[194:197]\n\t" \
  "s_cselect_b32 %[ao], 0, %[ao]\n\t" \
  "s_waitcnt lgkmcnt(6)\n\t" \
  "v_mfma_f32_16x16x32_bf16 v[78:81], v[58:61], v[34:37], v[78:81]\n\t" \
  "ds_read_b64_tr_b16 v[54:55], v19 offset:16384\n\t" \
  "v_mfma_f32_16x16x32_bf16 v[138:141], v[58:61], v[38:41], v[138:141]\n\t" \
  "ds_read_b64_tr_b16 v[56:57], v19 offset:18432\n\t" \
  "v_mfma_f32_16x16x32_bf16 v[198:201], v[58:61], v[42:45], v[198:201]\n\t" \
  "v_add_u32 v20, %[ao], v21\n\t" \
  "s_waitcnt lgkmcnt(6)\n\t" \
  "v_mfma_f32_16x16x32_bf16 v[82:85], v[62:65], v[34:37], v[82:85]\n\t" \
  "ds_read_b64_tr_b16 v[58:59], v12 offset:16640\n\t" \
  "v_mfma_f32_16x16x32_bf16 v[142:145], v[62:65], v[38:41], v[142:145]\n\t" \
  "ds_read_b64_tr_b16 v[60:61], v12 offset:18688\n\t" \
  "v_mfma_f32_16x16x32_bf16 v[202:205], v[62:65], v[42:45], v[202:205]\n\t" \
  "v_xor_b32 v19, 0x8000, v19\n\t" \
  "s_waitcnt lgkmcnt(6)\n\t" \
  "v_mfma_f32_16x16x32_bf16 v[86:89], v[46:49], v[34:37], v[86:89]\n\t" \
  "ds_read_b64_tr_b16 v[62:63], v13 offset:16640\n\t" \
  "v_mfma_f32_16x16x32_bf16 v[146:149], v[46:49], v[38:41], v[146:149]\n\t" \
  "ds_read_b64_tr_b16 v[64:65], v13 offset:18688\n\t" \
  "v_mfma_f32_16x16x32_bf16 v[206:209], v[46:49], v[42:45], v[206:209]\n\t" \
  "v_xor_b32 v12, 0x8000, v12\n\t" \
  "s_waitcnt lgkmcnt(6)\n\t" \
  "v_mfma_f32_16x16x32_bf16 v[90:93], v[50:53], v[34:37], v[90:93]\n\t" \
  "ds_read_b64_tr_b16 v[46:47], v14 offset:16640\n\t" \
  "v_mfma_f32_16x16x32_bf16 v[150:153], v[50:53], v[38:41], v[150:153]\n\t" \
  "ds_read_b64_tr_b16 v[48:49], v14 offset:18688\n\t" \
  "v_mfma_f32_16x16x32_bf16 v[210:213], v[50:53], v[42:45], v[210:213]\n\t" \
  "v_xor_b32 v13, 0x8000, v13\n\t" \
  "s_waitcnt lgkmcnt(6)\n\t" \
  "v_mfma_f32_16x16x32_bf16 v[94:97], v[54:57], v[34:37], v[94:97]\n\t" \
  "ds_read_b64_tr_b16 v[50:51], v15 offset:16640\n\t" \
  "v_mfma_f32_16x16x32_bf16 v[154:157], v[54:57], v[38:41], v[154:157]\n\t" \
  "ds_read_b64_tr_b16 v[52:53], v15 offset:18688\n\t" \
  "v_mfma_f32_16x16x32_bf16 v[214:217], v[54:57], v[42:45], v[214:217]\n\t" \
  "v_xor_b32 v14, 0x8000, v14\n\t" \
  "s_waitcnt lgkmcnt(6)\n\t" \
  "v_mfma_f32_16x16x32_bf16 v[98:101], v[58:61], v[34:37], v[98:101]\n\t" \
  "ds_read_b64_tr_b16 v[54:55], v16 offset:16640\n\t" \
  "v_mfma_f32_16x16x32_bf16 v[158:161], v[58:61], v[38:41], v[158:161]\n\t" \
  "ds_read_b64_tr_b16 v[56:57], v16 offset:18688\n\t" \
  "v_mfma_f32_16x16x32_bf16 v[218:221], v[58:61], v[42:45], v[218:221]\n\t" \
  "v_xor_b32 v15, 0x8000, v15\n\t" \
  "s_waitcnt lgkmcnt(6)\n\t" \
  "v_mfma_f32_16x16x32_bf16 v[102:105], v[62:65], v[34:37], v[102:105]\n\t" \
  "ds_read_b64_tr_b16 v[58:59], v17 offset:16640\n\t" \
  "v_mfma_f32_16x16x32_bf16 v[162:165], v[62:65], v[38:41], v[162:165]\n\t" \
  "ds_read_b64_tr_b16 v[60:61], v17 offset:18688\n\t" \
  "v_mfma_f32_16x16x32_bf16 v[222:225], v[62:65], v[42:45], v[222:225]\n\t" \
  "v_xor_b32 v16, 0x8000, v16\n\t" \
  "s_waitcnt lgkmcnt(6)\n\t" \
  "v_mfma_f32_16x16x32_bf16 v[106:109], v[34:37], v[46:49], v[106:109]\n\t" \
  "ds_read_b64_tr_b16 v[62:63], v18 offset:16640\n\t" \
  "v_mfma_f32_16x16x32_bf16 v[166:169], v[38:41], v[46:49], v[166:169]\n\t" \
  "ds_read_b64_tr_b16 v[64:65], v18 offset:18688\n\t" \
  "v_mfma_f32_16x16x32_bf16 v[226:229], v[42:45], v[46:49], v[226:229]\n\t" \
  "v_xor_b32 v17, 0x8000, v17\n\t" \
  "s_barrier\n\t" \
  "s_waitcnt lgkmcnt(6)\n\t" \
  "v_mfma_f32_16x16x32_bf16 v[110:113], v[34:37], v[50:53], v[110:113]\n\t" \
  "ds_read_b64_tr_b16 v[46:47], v12 offset:0\n\t" \
  "ds_read_b128 v[22:25], v20 offset:0\n\t" \
  "v_mfma_f32_16x16x32_bf16 v[170:173], v[38:41], v[50:53], v[170:173]\n\t" \
  "ds_read_b64_tr_b16 v[48:49], v12 offset:2048\n\t" \
  "ds_read_b128 v[26:29], v20 offset:2048\n\t" \
  "v_mfma_f32_16x16x32_bf16 v[230:233], v[42:45], v[50:53], v[230:233]\n\t" \
  "s_waitcnt lgkmcnt(8)\n\t" \
  "v_mfma_f32_16x16x32_bf16 v[114:117], v[34:37], v[54:57], v[114:117]\n\t" \
  "ds_read_b64_tr_b16 v[50:51], v13 offset:0\n\t" \
  "ds_read_b128 v[30:33], v20 offset:4096\n\t" \
  "v_mfma_f32_16x16x32_bf16 v[174:177], v[38:41], v[54:57], v[174:177]\n\t" \
  "ds_read_b64_tr_b16 v[52:53], v13 offset:2048\n\t" \
  "v_mfma_f32_16x16x32_bf16 v[234:237], v[42:45], v[54:57], v[234:237]\n\t" \
  "s_sub_u32 %[cnt], %[cnt], 1\n\t" \
  "s_waitcnt lgkmcnt(9)\n\t" \
  "v_mfma_f32_16x16x32_bf16 v[118:121], v[34:37], v[58:61], v[118:121]\n\t" \
  "ds_read_b64_tr_b16 v[54:55], v14 offset:0\n\t" \
  "v_mfma_f32_16x16x32_bf16 v[178:181], v[38:41], v[58:61], v[178:181]\n\t" \
  "ds_read_b64_tr_b16 v[56:57], v14 offset:2048\n\t" \
  "v_mfma_f32_16x16x32_bf16 v[238:241], v[42:45], v[58:61], v[238:241]\n\t" \
  "v_xor_b32 v18, 0x8000, v18\n\t" \
  "s_waitcnt lgkmcnt(9)\n\t" \
  "v_mfma_f32_16x16x32_bf16 v[122:125], v[34:37], v[62:65], v[122:125]\n\t" \
  "ds_read_b64_tr_b16 v[58:59], v15 offset:0\n\t" \
  "v_mfma_f32_16x16x32_bf16 v[182:185], v[38:41], v[62:65], v[182:185]\n\t" \
  "ds_read_b64_tr_b16 v[60:61], v15 offset:2048\n\t" \
  "v_mfma_f32_16x16x32_bf16 v[242:245], v[42:45], v[62:65], v[242:245]\n\t" \
  "s_cmp_lg_u32 %[cnt], 0\n\t" \
  "s_cbranch_scc1 Lt%=\n\t" \
  "s_waitcnt lgkmcnt(0)\n\t" \
  "s_nop 7\n\t" \
  "s_nop 1\n\t" \
  ""
#define S2_ALL \
  "s_waitcnt vmcnt(4)\n\t" \
  "s_lshr_b32 s40, s53, 4\n\t" \
  "s_add_u32 s40, s50, s40\n\t" \
  "s_addc_u32 s41, s51, 0\n\t" \
  "s_add_u32 m0, s57, 0\n\t" \
  "s_nop 0\n\t" \
  "global_load_lds_dwordx4 v5, s[40:41]\n\t" \
  "s_waitcnt vmcnt(4)\n\t" \
  "global_load_lds_dwordx4 v6, s[40:41] offset:1024\n\t" \
  "s_waitcnt vmcnt(4)\n\t" \
  "global_load_lds_dwordx4 v7, s[40:41] offset:2048\n\t" \
  "s_waitcnt vmcnt(4)\n\t" \
  "global_load_lds_dwordx4 v8, s[40:41] offset:3072\n\t" \
  "s_waitcnt vmcnt(4)\n\t" \
  "s_add_u32 s40, s40, 0xa000\n\t" \
  "s_addc_u32 s41, s41, 0\n\t" \
  "s_add_u32 m0, s57, 4096\n\t" \
  "s_nop 0\n\t" \
  "global_load_lds_dwordx4 v5, s[40:41]\n\t" \
  "s_waitcnt vmcnt(4)\n\t" \
  "global_load_lds_dwordx4 v6, s[40:41] offset:1024\n\t" \
  "s_waitcnt vmcnt(4)\n\t" \
  "global_load_lds_dwordx4 v7, s[40:41] offset:2048\n\t" \
  "s_waitcnt vmcnt(4)\n\t" \
  "global_load_lds_dwordx4 v8, s[40:41] offset:3072\n\t" \
  "s_waitcnt vmcnt(4)\n\t" \
  "s_add_u32 s40, s40, 0xa000\n\t" \
  "s_addc_u32 s41, s41, 0\n\t" \
  "s_add_u32 m0, s57, 8192\n\t" \
  "s_nop 0\n\t" \
  "global_load_lds_dwordx4 v5, s[40:41]\n\t" \
  "s_waitcnt vmcnt(4)\n\t" \
  "global_load_lds_dwordx4 v6, s[40:41] offset:1024\n\t" \
  "s_waitcnt vmcnt(4)\n\t" \
  "s_lshr_b32 s40, s54, 4\n\t" \
  "s_add_u32 s40, s50, s40\n\t" \
  "s_addc_u32 s41, s51, 0\n\t" \
  "s_add_u32 m0, s57, 20480\n\t" \
  "s_nop 0\n\t" \
  "global_load_lds_dwordx4 v5, s[40:41]\n\t" \
  "s_waitcnt vmcnt(4)\n\t" \
  "global_load_lds_dwordx4 v6, s[40:41] offset:1024\n\t" \
  "s_waitcnt vmcnt(4)\n\t" \
  "global_load_lds_dwordx4 v7, s[40:41] offset:2048\n\t" \
  "s_waitcnt vmcnt(4)\n\t" \
  "global_load_lds_dwordx4 v8, s[40:41] offset:3072\n\t" \
  "s_waitcnt vmcnt(4)\n\t" \
  "s_add_u32 s40, s40, 0xa000\n\t" \
  "s_addc_u32 s41, s41, 0\n\t" \
  "s_add_u32 m0, s57, 24576\n\t" \
  "s_nop 0\n\t" \
  "global_load_lds_dwordx4 v5, s[40:41]\n\t" \
  "s_waitcnt vmcnt(4)\n\t" \
  "global_load_lds_dwordx4 v6, s[40:41] offset:1024\n\t" \
  "s_waitcnt vmcnt(4)\n\t" \
  "global_load_lds_dwordx4 v7, s[40:41] offset:2048\n\t" \
  "s_waitcnt vmcnt(4)\n\t" \
  "global_load_lds_dwordx4 v8, s[40:41] offset:3072\n\t" \
  "s_waitcnt vmcnt(4)\n\t" \
  "s_add_u32 s40, s40, 0xa000\n\t" \
  "s_addc_u32 s41, s41, 0\n\t" \
  "s_add_u32 m0, s57, 28672\n\t" \
  "s_nop 0\n\t" \
  "global_load_lds_dwordx4 v5, s[40:41]\n\t" \
  "s_waitcnt vmcnt(4)\n\t" \
  "global_load_lds_dwordx4 v6, s[40:41] offset:1024\n\t" \
  "s_waitcnt vmcnt(4)\n\t" \
  "s_add_u32 s40, s58, 0\n\t" \
  "s_addc_u32 s41, s59, 0\n\t" \
  "s_add_u32 m0, s57, 30720\n\t" \
  "s_nop 0\n\t" \
  "global_load_lds_dwordx4 v232, s[40:41]\n\t" \
  "s_waitcnt vmcnt(4)\n\t" \
  "global_load_lds_dwordx4 v233, s[40:41] offset:1024\n\t" \
  "s_waitcnt vmcnt(4)\n\t" \
  "global_load_lds_dwordx4 v234, s[40:41] offset:2048\n\t" \
  "s_waitcnt vmcnt(4)\n\t" \
  "global_load_lds_dwordx4 v235, s[40:41] offset:3072\n\t" \
  "s_waitcnt vmcnt(4)\n\t" \
  "s_add_u32 m0, s57, 34816\n\t" \
  "s_nop 0\n\t" \
  "global_load_lds_dwordx4 v236, s[40:41]\n\t" \
  "ds_read_b64 v[96:97], v9 offset:10240\n\t" \
  "ds_read_b64 v[98:99], v10 offset:10240\n\t" \
  "ds_read_b64 v[100:101], v9 offset:12288\n\t" \
  "ds_read_b64 v[102:103], v10 offset:12288\n\t" \
  "ds_read_b64 v[104:105], v9 offset:14336\n\t" \
  "ds_read_b64 v[106:107], v10 offset:14336\n\t" \
  "ds_read_b64 v[108:109], v9 offset:16384\n\t" \
  "ds_read_b64 v[110:111], v10 offset:16384\n\t" \
  "ds_read_b64 v[112:113], v9 offset:18432\n\t" \
  "ds_read_b64 v[114:115], v10 offset:18432\n\t" \
  "s_waitcnt lgkmcnt(0)\n\t" \
  "ds_read_b64 v[116:117], v11 offset:10240\n\t" \
  "ds_read_b64 v[118:119], v12 offset:10240\n\t" \
  "ds_read_b64 v[120:121], v11 offset:12288\n\t" \
  "ds_read_b64 v[122:123], v12 offset:12288\n\t" \
  "ds_read_b64 v[124:125], v11 offset:14336\n\t" \
  "ds_read_b64 v[126:127], v12 offset:14336\n\t" \
  "ds_read_b64 v[128:129], v11 offset:16384\n\t" \
  "ds_read_b64 v[130:131], v12 offset:16384\n\t" \
  "ds_read_b64 v[132:133], v11 offset:18432\n\t" \
  "ds_read_b64 v[134:135], v12 offset:18432\n\t" \
  "s_waitcnt lgkmcnt(0)\n\t" \
  "s_waitcnt vmcnt(4)\n\t" \
  "s_lshr_b32 s40, s55, 4\n\t" \
  "s_add_u32 s40, s50, s40\n\t" \
  "s_addc_u32 s41, s51, 0\n\t" \
  "s_add_u32 m0, s57, 10240\n\t" \
  "s_nop 0\n\t" \
  "global_load_lds_dwordx4 v5, s[40:41]\n\t" \
  "s_waitcnt vmcnt(4)\n\t" \
  "global_load_lds_dwordx4 v6, s[40:41] offset:1024\n\t" \
  "s_waitcnt vmcnt(4)\n\t" \
  "global_load_lds_dwordx4 v7, s[40:41] offset:2048\n\t" \
  "s_waitcnt vmcnt(4)\n\t" \
  "global_load_lds_dwordx4 v8, s[40:41] offset:3072\n\t" \
  "s_waitcnt vmcnt(4)\n\t" \
  "s_add_u32 s40, s40, 0xa000\n\t" \
  "s_addc_u32 s41, s41, 0\n\t" \
  "s_add_u32 m0, s57, 14336\n\t" \
  "s_nop 0\n\t" \
  "global_load_lds_dwordx4 v5, s[40:41]\n\t" \
  "s_waitcnt vmcnt(4)\n\t" \
  "global_load_lds_dwordx4 v6, s[40:41] offset:1024\n\t" \
  "s_waitcnt vmcnt(4)\n\t" \
  "global_load_lds_dwordx4 v7, s[40:41] offset:2048\n\t" \
  "s_waitcnt vmcnt(4)\n\t" \
  "global_load_lds_dwordx4 v8, s[40:41] offset:3072\n\t" \
  "s_waitcnt vmcnt(4)\n\t" \
  "s_add_u32 s40, s40, 0xa000\n\t" \
  "s_addc_u32 s41, s41, 0\n\t" \
  "s_add_u32 m0, s57, 18432\n\t" \
  "s_nop 0\n\t" \
  "global_load_lds_dwordx4 v5, s[40:41]\n\t" \
  "s_waitcnt vmcnt(4)\n\t" \
  "global_load_lds_dwordx4 v6, s[40:41] offset:1024\n\t" \
  "s_mov_b32 s40, 0x1000000\n\t" \
  "Ld%=:\n\t" \
  "ds_read_b32 v13, v241 offset:4\n\t" \
  "s_waitcnt lgkmcnt(0)\n\t" \
  "v_readfirstlane_b32 s41, v13\n\t" \
  "s_sub_u32 s40, s40, 1\n\t" \
  "s_cselect_b32 s41, 1, s41\n\t" \
  "s_cmp_lg_u32 s41, 0\n\t" \
  "s_cbranch_scc1 Ldx%=\n\t" \
  "s_sleep 1\n\t" \
  "s_branch Ld%=\n\t" \
  "Ldx%=:\n\t" \
  "v_add_u32 v13, s52, v2\n\t" \
  "global_load_dwordx4 v[136:139], v13, s[42:43] offset:0 sc1\n\t" \
  "global_load_dwordx4 v[140:143], v13, s[44:45] offset:0 sc1\n\t" \
  "global_load_dwordx4 v[144:147], v13, s[46:47] offset:0 sc1\n\t" \
  "global_load_dwordx4 v[148:151], v13, s[48:49] offset:0 sc1\n\t" \
  "global_load_dwordx4 v[152:155], v13, s[42:43] offset:1024 sc1\n\t" \
  "global_load_dwordx4 v[156:159], v13, s[44:45] offset:1024 sc1\n\t" \
  "global_load_dwordx4 v[160:163], v13, s[46:47] offset:1024 sc1\n\t" \
  "global_load_dwordx4 v[164:167], v13, s[48:49] offset:1024 sc1\n\t" \
  "v_add_u32 v13, s53, v2\n\t" \
  "global_load_dwordx4 v[168:171], v13, s[42:43] offset:0 sc1\n\t" \
  "global_load_dwordx4 v[172:175], v13, s[44:45] offset:0 sc1\n\t" \
  "global_load_dwordx4 v[176:179], v13, s[46:47] offset:0 sc1\n\t" \
  "global_load_dwordx4 v[180:183], v13, s[48:49] offset:0 sc1\n\t" \
  "global_load_dwordx4 v[184:187], v13, s[42:43] offset:1024 sc1\n\t" \
  "global_load_dwordx4 v[188:191], v13, s[44:45] offset:1024 sc1\n\t" \
  "global_load_dwordx4 v[192:195], v13, s[46:47] offset:1024 sc1\n\t" \
  "global_load_dwordx4 v[196:199], v13, s[48:49] offset:1024 sc1\n\t" \
  "s_waitcnt vmcnt(15)\n\t" \
  "v_mfma_f32_16x16x32_bf16 v[16:19], v[96:99], v[136:139], 0\n\t" \
  "s_waitcnt vmcnt(14)\n\t" \
  "v_mfma_f32_16x16x32_bf16 v[36:39], v[96:99], v[140:143], 0\n\t" \
  "s_waitcnt vmcnt(13)\n\t" \
  "v_mfma_f32_16x16x32_bf16 v[56:59], v[96:99], v[144:147], 0\n\t" \
  "v_add_u32 v13, s54, v2\n\t" \
  "global_load_dwordx4 v[200:203], v13, s[42:43] offset:0 sc1\n\t" \
  "s_waitcnt vmcnt(13)\n\t" \
  "v_mfma_f32_16x16x32_bf16 v[76:79], v[96:99], v[148:151], 0\n\t" \
  "v_mfma_f32_16x16x32_bf16 v[20:23], v[100:103], v[136:139], 0\n\t" \
  "v_mfma_f32_16x16x32_bf16 v[40:43], v[100:103], v[140:143], 0\n\t" \
  "global_load_dwordx4 v[204:207], v13, s[44:45] offset:0 sc1\n\t" \
  "v_mfma_f32_16x16x32_bf16 v[60:63], v[100:103], v[144:147], 0\n\t" \
  "v_mfma_f32_16x16x32_bf16 v[80:83], v[100:103], v[148:151], 0\n\t" \
  "v_mfma_f32_16x16x32_bf16 v[24:27], v[104:107], v[136:139], 0\n\t" \
  "global_load_dwordx4 v[208:211], v13, s[46:47] offset:0 sc1\n\t" \
  "v_mfma_f32_16x16x32_bf16 v[44:47], v[104:107], v[140:143], 0\n\t" \
  "v_mfma_f32_16x16x32_bf16 v[64:67], v[104:107], v[144:147], 0\n\t" \
  "v_mfma_f32_16x16x32_bf16 v[84:87], v[104:107], v[148:151], 0\n\t" \
  "global_load_dwordx4 v[212:215], v13, s[48:49] offset:0 sc1\n\t" \
  "v_mfma_f32_16x16x32_bf16 v[28:31], v[108:111], v[136:139], 0\n\t" \
  "v_mfma_f32_16x16x32_bf16 v[48:51], v[108:111], v[140:143], 0\n\t" \
  "v_mfma_f32_16x16x32_bf16 v[68:71], v[108:111], v[144:147], 0\n\t" \
  "global_load_dwordx4 v[216:219], v13, s[42:43] offset:1024 sc1\n\t" \
  "v_mfma_f32_16x16x32_bf16 v[88:91], v[108:111], v[148:151], 0\n\t" \
  "v_mfma_f32_16x16x32_bf16 v[32:35], v[112:115], v[136:139], 0\n\t" \
  "v_mfma_f32_16x16x32_bf16 v[52:55], v[112:115], v[140:143], 0\n\t" \
  "global_load_dwordx4 v[220:223], v13, s[44:45] offset:1024 sc1\n\t" \
  "v_mfma_f32_16x16x32_bf16 v[72:75], v[112:115], v[144:147], 0\n\t" \
  "v_mfma_f32_16x16x32_bf16 v[92:95], v[112:115], v[148:151], 0\n\t" \
  "s_waitcnt vmcnt(17)\n\t" \
  "v_mfma_f32_16x16x32_bf16 v[16:19], v[116:119], v[152:155], v[16:19]\n\t" \
  "ds_read_b64 v[96:97], v9 offset:0\n\t" \
  "global_load_dwordx4 v[224:227], v13, s[46:47] offset:1024 sc1\n\t" \
  "s_waitcnt vmcnt(17)\n\t" \
  "v_mfma_f32_16x16x32_bf16 v[36:39], v[116:119], v[156:159], v[36:39]\n\t" \
  "ds_read_b64 v[98:99], v10 offset:0\n\t" \
  "s_waitcnt vmcnt(16)\n\t" \
  "v_mfma_f32_16x16x32_bf16 v[56:59], v[116:119], v[160:163], v[56:59]\n\t" \
  "ds_read_b64 v[100:101], v9 offset:2048\n\t" \
  "s_waitcnt vmcnt(15)\n\t" \
  "v_mfma_f32_16x16x32_bf16 v[76:79], v[116:119], v[164:167], v[76:79]\n\t" \
  "ds_read_b64 v[102:103], v10 offset:2048\n\t" \
  "global_load_dwordx4 v[228:231], v13, s[48:49] offset:1024 sc1\n\t" \
  "v_mfma_f32_16x16x32_bf16 v[20:23], v[120:123], v[152:155], v[20:23]\n\t" \
  "ds_read_b64 v[104:105], v9 offset:4096\n\t" \
  "v_mfma_f32_16x16x32_bf16 v[40:43], v[120:123], v[156:159], v[40:43]\n\t" \
  "ds_read_b64 v[106:107], v10 offset:4096\n\t" \
  "v_mfma_f32_16x16x32_bf16 v[60:63], v[120:123], v[160:163], v[60:63]\n\t" \
  "ds_read_b64 v[108:109], v9 offset:6144\n\t" \
  "v_mfma_f32_16x16x32_bf16 v[80:83], v[120:123], v[164:167], v[80:83]\n\t" \
  "ds_read_b64 v[110:111], v10 offset:6144\n\t" \
  "v_mfma_f32_16x16x32_bf16 v[24:27], v[124:127], v[152:155], v[24:27]\n\t" \
  "ds_read_b64 v[112:113], v9 offset:8192\n\t" \
  "v_mfma_f32_16x16x32_bf16 v[44:47], v[124:127], v[156:159], v[44:47]\n\t" \
  "ds_read_b64 v[114:115], v10 offset:8192\n\t" \
  "v_mfma_f32_16x16x32_bf16 v[64:67], v[124:127], v[160:163], v[64:67]\n\t" \
  "v_mfma_f32_16x16x32_bf16 v[84:87], v[124:127], v[164:167], v[84:87]\n\t" \
  "v_mfma_f32_16x16x32_bf16 v[28:31], v[128:131], v[152:155], v[28:31]\n\t" \
  "v_mfma_f32_16x16x32_bf16 v[48:51], v[128:131], v[156:159], v[48:51]\n\t" \
  "v_mfma_f32_16x16x32_bf16 v[68:71], v[128:131], v[160:163], v[68:71]\n\t" \
  "v_mfma_f32_16x16x32_bf16 v[88:91], v[128:131], v[164:167], v[88:91]\n\t" \
  "v_mfma_f32_16x16x32_bf16 v[32:35], v[132:135], v[152:155], v[32:35]\n\t" \
  "v_mfma_f32_16x16x32_bf16 v[52:55], v[132:135], v[156:159], v[52:55]\n\t" \
  "v_mfma_f32_16x16x32_bf16 v[72:75], v[132:135], v[160:163], v[72:75]\n\t" \
  "v_mfma_f32_16x16x32_bf16 v[92:95], v[132:135], v[164:167], v[92:95]\n\t" \
  "s_waitcnt vmcnt(15)\n\t" \
  "s_waitcnt lgkmcnt(8)\n\t" \
  "v_mfma_f32_16x16x32_bf16 v[16:19], v[96:99], v[168:171], v[16:19]\n\t" \
  "ds_read_b64 v[116:117], v11 offset:0\n\t" \
  "v_add_u32 v13, s55, v2\n\t" \
  "global_load_dwordx4 v[136:139], v13, s[42:43] offset:0 sc1\n\t" \
  "s_waitcnt vmcnt(15)\n\t" \
  "v_mfma_f32_16x16x32_bf16 v[36:39], v[96:99], v[172:175], v[36:39]\n\t" \
  "ds_read_b64 v[118:119], v12 offset:0\n\t" \
  "s_waitcnt vmcnt(14)\n\t" \
  "v_mfma_f32_16x16x32_bf16 v[56:59], v[96:99], v[176:179], v[56:59]\n\t" \
  "ds_read_b64 v[120:121], v11 offset:2048\n\t" \
  "s_waitcnt vmcnt(13)\n\t" \
  "v_mfma_f32_16x16x32_bf16 v[76:79], v[96:99], v[180:183], v[76:79]\n\t" \
  "ds_read_b64 v[122:123], v12 offset:2048\n\t" \
  "global_load_dwordx4 v[140:143], v13, s[44:45] offset:0 sc1\n\t" \
  "s_waitcnt lgkmcnt(10)\n\t" \
  "v_mfma_f32_16x16x32_bf16 v[20:23], v[100:103], v[168:171], v[20:23]\n\t" \
  "ds_read_b64 v[124:125], v11 offset:4096\n\t" \
  "v_mfma_f32_16x16x32_bf16 v[40:43], v[100:103], v[172:175], v[40:43]\n\t" \
  "ds_read_b64 v[126:127], v12 offset:4096\n\t" \
  "v_mfma_f32_16x16x32_bf16 v[60:63], v[100:103], v[176:179], v[60:63]\n\t" \
  "ds_read_b64 v[128:129], v11 offset:6144\n\t" \
  "global_load_dwordx4 v[144:147], v13, s[46:47] offset:0 sc1\n\t" \
  "v_mfma_f32_16x16x32_bf16 v[80:83], v[100:103], v[180:183], v[80:83]\n\t" \
  "ds_read_b64 v[130:131], v12 offset:6144\n\t" \
  "s_waitcnt lgkmcnt(12)\n\t" \
  "v_mfma_f32_16x16x32_bf16 v[24:27], v[104:107], v[168:171], v[24:27]\n\t" \
  "ds_read_b64 v[132:133], v11 offset:8192\n\t" \
  "v_mfma_f32_16x16x32_bf16 v[44:47], v[104:107], v[172:175], v[44:47]\n\t" \
  "ds_read_b64 v[134:135], v12 offset:8192\n\t" \
  "global_load_dwordx4 v[148:151], v13, s[48:49] offset:0 sc1\n\t" \
  "v_mfma_f32_16x16x32_bf16 v[64:67], v[104:107], v[176:179], v[64:67]\n\t" \
  "v_mfma_f32_16x16x32_bf16 v[84:87], v[104:107], v[180:183], v[84:87]\n\t" \
  "s_waitcnt lgkmcnt(12)\n\t" \
  "v_mfma_f32_16x16x32_bf16 v[28:31], v[108:111], v[168:171], v[28:31]\n\t" \
  "global_load_dwordx4 v[152:155], v13, s[42:43] offset:1024 sc1\n\t" \
  "v_mfma_f32_16x16x32_bf16 v[48:51], v[108:111], v[172:175], v[48:51]\n\t" \
  "v_mfma_f32_16x16x32_bf16 v[68:71], v[108:111], v[176:179], v[68:71]\n\t" \
  "v_mfma_f32_16x16x32_bf16 v[88:91], v[108:111], v[180:183], v[88:91]\n\t" \
  "global_load_dwordx4 v[156:159], v13, s[44:45] offset:1024 sc1\n\t" \
  "s_waitcnt lgkmcnt(10)\n\t" \
  "v_mfma_f32_16x16x32_bf16 v[32:35], v[112:115], v[168:171], v[32:35]\n\t" \
  "v_mfma_f32_16x16x32_bf16 v[52:55], v[112:115], v[172:175], v[52:55]\n\t" \
  "v_mfma_f32_16x16x32_bf16 v[72:75], v[112:115], v[176:179], v[72:75]\n\t" \
  "global_load_dwordx4 v[160:163], v13, s[46:47] offset:1024 sc1\n\t" \
  "v_mfma_f32_16x16x32_bf16 v[92:95], v[112:115], v[180:183], v[92:95]\n\t" \
  "s_waitcnt vmcnt(18)\n\t" \
  "s_waitcnt lgkmcnt(8)\n\t" \
  "v_mfma_f32_16x16x32_bf16 v[16:19], v[116:119], v[184:187], v[16:19]\n\t" \
  "ds_read_b64 v[96:97], v9 offset:20480\n\t" \
  "s_waitcnt vmcnt(17)\n\t" \
  "v_mfma_f32_16x16x32_bf16 v[36:39], v[116:119], v[188:191], v[36:39]\n\t" \
  "ds_read_b64 v[98:99], v10 offset:20480\n\t" \
  "global_load_dwordx4 v[164:167], v13, s[48:49] offset:1024 sc1\n\t" \
  "s_waitcnt vmcnt(17)\n\t" \
  "v_mfma_f32_16x16x32_bf16 v[56:59], v[116:119], v[192:195], v[56:59]\n\t" \
  "ds_read_b64 v[100:101], v9 offset:22528\n\t" \
  "s_waitcnt vmcnt(16)\n\t" \
  "v_mfma_f32_16x16x32_bf16 v[76:79], v[116:119], v[196:199], v[76:79]\n\t" \
  "ds_read_b64 v[102:103], v10 offset:22528\n\t" \
  "s_waitcnt lgkmcnt(10)\n\t" \
  "v_mfma_f32_16x16x32_bf16 v[20:23], v[120:123], v[184:187], v[20:23]\n\t" \
  "ds_read_b64 v[104:105], v9 offset:24576\n\t" \
  "s_waitcnt lgkmcnt(5)\n\t" \
  "s_lshr_b32 s40, s56, 4\n\t" \
  "s_add_u32 s40, s50, s40\n\t" \
  "s_addc_u32 s41, s51, 0\n\t" \
  "s_add_u32 m0, s57, 0\n\t" \
  "s_nop 0\n\t" \
  "global_load_lds_dwordx4 v5, s[40:41]\n\t" \
  "v_mfma_f32_16x16x32_bf16 v[40:43], v[120:123], v[188:191], v[40:43]\n\t" \
  "ds_read_b64 v[106:107], v10 offset:24576\n\t" \
  "v_mfma_f32_16x16x32_bf16 v[60:63], v[120:123], v[192:195], v[60:63]\n\t" \
  "ds_read_b64 v[108:109], v9 offset:26624\n\t" \
  "v_mfma_f32_16x16x32_bf16 v[80:83], v[120:123], v[196:199], v[80:83]\n\t" \
  "ds_read_b64 v[110:111], v10 offset:26624\n\t" \
  "global_load_lds_dwordx4 v6, s[40:41] offset:1024\n\t" \
  "v_mfma_f32_16x16x32_bf16 v[24:27], v[124:127], v[184:187], v[24:27]\n\t" \
  "ds_read_b64 v[112:113], v9 offset:28672\n\t" \
  "v_mfma_f32_16x16x32_bf16 v[44:47], v[124:127], v[188:191], v[44:47]\n\t" \
  "ds_read_b64 v[114:115], v10 offset:28672\n\t" \
  "v_mfma_f32_16x16x32_bf16 v[64:67], v[124:127], v[192:195], v[64:67]\n\t" \
  "global_load_lds_dwordx4 v7, s[40:41] offset:2048\n\t" \
  "v_mfma_f32_16x16x32_bf16 v[84:87], v[124:127], v[196:199], v[84:87]\n\t" \
  "v_mfma_f32_16x16x32_bf16 v[28:31], v[128:131], v[184:187], v[28:31]\n\t" \
  "v_mfma_f32_16x16x32_bf16 v[48:51], v[128:131], v[188:191], v[48:51]\n\t" \
  "global_load_lds_dwordx4 v8, s[40:41] offset:3072\n\t" \
  "v_mfma_f32_16x16x32_bf16 v[68:71], v[128:131], v[192:195], v[68:71]\n\t" \
  "v_mfma_f32_16x16x32_bf16 v[88:91], v[128:131], v[196:199], v[88:91]\n\t" \
  "v_mfma_f32_16x16x32_bf16 v[32:35], v[132:135], v[184:187], v[32:35]\n\t" \
  "s_add_u32 s40, s40, 0xa000\n\t" \
  "s_addc_u32 s41, s41, 0\n\t" \
  "s_add_u32 m0, s57, 4096\n\t" \
  "s_nop 0\n\t" \
  "global_load_lds_dwordx4 v5, s[40:41]\n\t" \
  "v_mfma_f32_16x16x32_bf16 v[52:55], v[132:135], v[188:191], v[52:55]\n\t" \
  "v_mfma_f32_16x16x32_bf16 v[72:75], v[132:135], v[192:195], v[72:75]\n\t" \
  "v_mfma_f32_16x16x32_bf16 v[92:95], v[132:135], v[196:199], v[92:95]\n\t" \
  "global_load_lds_dwordx4 v6, s[40:41] offset:1024\n\t" \
  "s_waitcnt vmcnt(21)\n\t" \
  "s_waitcnt lgkmcnt(8)\n\t" \
  "v_mfma_f32_16x16x32_bf16 v[16:19], v[96:99], v[200:203], v[16:19]\n\t" \
  "ds_read_b64 v[116:117], v11 offset:20480\n\t" \
  "s_waitcnt vmcnt(20)\n\t" \
  "v_mfma_f32_16x16x32_bf16 v[36:39], v[96:99], v[204:207], v[36:39]\n\t" \
  "ds_read_b64 v[118:119], v12 offset:20480\n\t" \
  "s_waitcnt vmcnt(19)\n\t" \
  "v_mfma_f32_16x16x32_bf16 v[56:59], v[96:99], v[208:211], v[56:59]\n\t" \
  "ds_read_b64 v[120:121], v11 offset:22528\n\t" \
  "global_load_lds_dwordx4 v7, s[40:41] offset:2048\n\t" \
  "s_waitcnt vmcnt(19)\n\t" \
  "v_mfma_f32_16x16x32_bf16 v[76:79], v[96:99], v[212:215], v[76:79]\n\t" \
  "ds_read_b64 v[122:123], v12 offset:22528\n\t" \
  "s_waitcnt lgkmcnt(10)\n\t" \
  "v_mfma_f32_16x16x32_bf16 v[20:23], v[100:103], v[200:203], v[20:23]\n\t" \
  "ds_read_b64 v[124:125], v11 offset:24576\n\t" \
  "v_mfma_f32_16x16x32_bf16 v[40:43], v[100:103], v[204:207], v[40:43]\n\t" \
  "ds_read_b64 v[126:127], v12 offset:24576\n\t" \
  "global_load_lds_dwordx4 v8, s[40:41] offset:3072\n\t" \
  "v_mfma_f32_16x16x32_bf16 v[60:63], v[100:103], v[208:211], v[60:63]\n\t" \
  "ds_read_b64 v[128:129], v11 offset:26624\n\t" \
  "v_mfma_f32_16x16x32_bf16 v[80:83], v[100:103], v[212:215], v[80:83]\n\t" \
  "ds_read_b64 v[130:131], v12 offset:26624\n\t" \
  "s_waitcnt lgkmcnt(12)\n\t" \
  "v_mfma_f32_16x16x32_bf16 v[24:27], v[104:107], v[200:203], v[24:27]\n\t" \
  "ds_read_b64 v[132:133], v11 offset:28672\n\t" \
  "s_add_u32 s40, s40, 0xa000\n\t" \
  "s_addc_u32 s41, s41, 0\n\t" \
  "s_add_u32 m0, s57, 8192\n\t" \
  "s_nop 0\n\t" \
  "global_load_lds_dwordx4 v5, s[40:41]\n\t" \
  "v_mfma_f32_16x16x32_bf16 v[44:47], v[104:107], v[204:207], v[44:47]\n\t" \
  "ds_read_b64 v[134:135], v12 offset:28672\n\t" \
  "v_mfma_f32_16x16x32_bf16 v[64:67], v[104:107], v[208:211], v[64:67]\n\t" \
  "v_mfma_f32_16x16x32_bf16 v[84:87], v[104:107], v[212:215], v[84:87]\n\t" \
  "global_load_lds_dwordx4 v6, s[40:41] offset:1024\n\t" \
  "s_waitcnt lgkmcnt(12)\n\t" \
  "v_mfma_f32_16x16x32_bf16 v[28:31], v[108:111], v[200:203], v[28:31]\n\t" \
  "v_mfma_f32_16x16x32_bf16 v[48:51], v[108:111], v[204:207], v[48:51]\n\t" \
  "v_mfma_f32_16x16x32_bf16 v[68:71], v[108:111], v[208:211], v[68:71]\n\t" \
  "v_add_u32 v13, s56, v2\n\t" \
  "global_load_dwordx4 v[168:171], v13, s[42:43] offset:0 sc1\n\t" \
  "v_mfma_f32_16x16x32_bf16 v[88:91], v[108:111], v[212:215], v[88:91]\n\t" \
  "s_waitcnt lgkmcnt(10)\n\t" \
  "v_mfma_f32_16x16x32_bf16 v[32:35], v[112:115], v[200:203], v[32:35]\n\t" \
  "v_mfma_f32_16x16x32_bf16 v[52:55], v[112:115], v[204:207], v[52:55]\n\t" \
  "global_load_dwordx4 v[172:175], v13, s[44:45] offset:0 sc1\n\t" \
  "v_mfma_f32_16x16x32_bf16 v[72:75], v[112:115], v[208:211], v[72:75]\n\t" \
  "v_mfma_f32_16x16x32_bf16 v[92:95], v[112:115], v[212:215], v[92:95]\n\t" \
  "s_waitcnt vmcnt(23)\n\t" \
  "s_waitcnt lgkmcnt(8)\n\t" \
  "v_mfma_f32_16x16x32_bf16 v[16:19], v[116:119], v[216:219], v[16:19]\n\t" \
  "ds_read_b64 v[96:97], v9 offset:10240\n\t" \
  "global_load_dwordx4 v[176:179], v13, s[46:47] offset:0 sc1\n\t" \
  "s_waitcnt vmcnt(23)\n\t" \
  "v_mfma_f32_16x16x32_bf16 v[36:39], v[116:119], v[220:223], v[36:39]\n\t" \
  "ds_read_b64 v[98:99], v10 offset:10240\n\t" \
  "s_waitcnt vmcnt(22)\n\t" \
  "v_mfma_f32_16x16x32_bf16 v[56:59], v[116:119], v[224:227], v[56:59]\n\t" \
  "ds_read_b64 v[100:101], v9 offset:12288\n\t" \
  "s_waitcnt vmcnt(21)\n\t" \
  "v_mfma_f32_16x16x32_bf16 v[76:79], v[116:119], v[228:231], v[76:79]\n\t" \
  "ds_read_b64 v[102:103], v10 offset:12288\n\t" \
  "global_load_dwordx4 v[180:183], v13, s[48:49] offset:0 sc1\n\t" \
  "s_waitcnt lgkmcnt(10)\n\t" \
  "v_mfma_f32_16x16x32_bf16 v[20:23], v[120:123], v[216:219], v[20:23]\n\t" \
  "ds_read_b64 v[104:105], v9 offset:14336\n\t" \
  "v_mfma_f32_16x16x32_bf16 v[40:43], v[120:123], v[220:223], v[40:43]\n\t" \
  "ds_read_b64 v[106:107], v10 offset:14336\n\t" \
  "v_mfma_f32_16x16x32_bf16 v[60:63], v[120:123], v[224:227], v[60:63]\n\t" \
  "ds_read_b64 v[108:109], v9 offset:16384\n\t" \
  "global_load_dwordx4 v[184:187], v13, s[42:43] offset:1024 sc1\n\t" \
  "v_mfma_f32_16x16x32_bf16 v[80:83], v[120:123], v[228:231], v[80:83]\n\t" \
  "ds_read_b64 v[110:111], v10 offset:16384\n\t" \
  "s_waitcnt lgkmcnt(12)\n\t" \
  "v_mfma_f32_16x16x32_bf16 v[24:27], v[124:127], v[216:219], v[24:27]\n\t" \
  "ds_read_b64 v[112:113], v9 offset:18432\n\t" \
  "v_mfma_f32_16x16x32_bf16 v[44:47], v[124:127], v[220:223], v[44:47]\n\t" \
  "ds_read_b64 v[114:115], v10 offset:18432\n\t" \
  "global_load_dwordx4 v[188:191], v13, s[44:45] offset:1024 sc1\n\t" \
  "v_mfma_f32_16x16x32_bf16 v[64:67], v[124:127], v[224:227], v[64:67]\n\t" \
  "v_mfma_f32_16x16x32_bf16 v[84:87], v[124:127], v[228:231], v[84:87]\n\t" \
  "s_waitcnt lgkmcnt(12)\n\t" \
  "v_mfma_f32_16x16x32_bf16 v[28:31], v[128:131], v[216:219], v[28:31]\n\t" \
  "global_load_dwordx4 v[192:195], v13, s[46:47] offset:1024 sc1\n\t" \
  "v_mfma_f32_16x16x32_bf16 v[48:51], v[128:131], v[220:223], v[48:51]\n\t" \
  "v_mfma_f32_16x16x32_bf16 v[68:71], v[128:131], v[224:227], v[68:71]\n\t" \
  "v_mfma_f32_16x16x32_bf16 v[88:91], v[128:131], v[228:231], v[88:91]\n\t" \
  "global_load_dwordx4 v[196:199], v13, s[48:49] offset:1024 sc1\n\t" \
  "s_waitcnt lgkmcnt(10)\n\t" \
  "v_mfma_f32_16x16x32_bf16 v[32:35], v[132:135], v[216:219], v[32:35]\n\t" \
  "v_mfma_f32_16x16x32_bf16 v[52:55], v[132:135], v[220:223], v[52:55]\n\t" \
  "v_mfma_f32_16x16x32_bf16 v[72:75], v[132:135], v[224:227], v[72:75]\n\t" \
  "s_add_u32 s40, s58, 40960\n\t" \
  "s_addc_u32 s41, s59, 0\n\t" \
  "s_add_u32 m0, s57, 20480\n\t" \
  "s_nop 0\n\t" \
  "global_load_lds_dwordx4 v232, s[40:41]\n\t" \
  "v_mfma_f32_16x16x32_bf16 v[92:95], v[132:135], v[228:231], v[92:95]\n\t" \
  "s_waitcnt vmcnt(26)\n\t" \
  "s_waitcnt lgkmcnt(8)\n\t" \
  "v_mfma_f32_16x16x32_bf16 v[16:19], v[96:99], v[136:139], v[16:19]\n\t" \
  "ds_read_b64 v[116:117], v11 offset:10240\n\t" \
  "s_waitcnt vmcnt(25)\n\t" \
  "v_mfma_f32_16x16x32_bf16 v[36:39], v[96:99], v[140:143], v[36:39]\n\t" \
  "ds_read_b64 v[118:119], v12 offset:10240\n\t" \
  "global_load_lds_dwordx4 v233, s[40:41] offset:1024\n\t" \
  "s_waitcnt vmcnt(25)\n\t" \
  "v_mfma_f32_16x16x32_bf16 v[56:59], v[96:99], v[144:147], v[56:59]\n\t" \
  "ds_read_b64 v[120:121], v11 offset:12288\n\t" \
  "s_waitcnt vmcnt(24)\n\t" \
  "v_mfma_f32_16x16x32_bf16 v[76:79], v[96:99], v[148:151], v[76:79]\n\t" \
  "ds_read_b64 v[122:123], v12 offset:12288\n\t" \
  "s_waitcnt lgkmcnt(10)\n\t" \
  "v_mfma_f32_16x16x32_bf16 v[20:23], v[100:103], v[136:139], v[20:23]\n\t" \
  "ds_read_b64 v[124:125], v11 offset:14336\n\t" \
  "global_load_lds_dwordx4 v234, s[40:41] offset:2048\n\t" \
  "v_mfma_f32_16x16x32_bf16 v[40:43], v[100:103], v[140:143], v[40:43]\n\t" \
  "ds_read_b64 v[126:127], v12 offset:14336\n\t" \
  "v_mfma_f32_16x16x32_bf16 v[60:63], v[100:103], v[144:147], v[60:63]\n\t" \
  "ds_read_b64 v[128:129], v11 offset:16384\n\t" \
  "v_mfma_f32_16x16x32_bf16 v[80:83], v[100:103], v[148:151], v[80:83]\n\t" \
  "ds_read_b64 v[130:131], v12 offset:16384\n\t" \
  "global_load_lds_dwordx4 v235, s[40:41] offset:3072\n\t" \
  "s_waitcnt lgkmcnt(12)\n\t" \
  "v_mfma_f32_16x16x32_bf16 v[24:27], v[104:107], v[136:139], v[24:27]\n\t" \
  "ds_read_b64 v[132:133], v11 offset:18432\n\t" \
  "v_mfma_f32_16x16x32_bf16 v[44:47], v[104:107], v[140:143], v[44:47]\n\t" \
  "ds_read_b64 v[134:135], v12 offset:18432\n\t" \
  "v_mfma_f32_16x16x32_bf16 v[64:67], v[104:107], v[144:147], v[64:67]\n\t" \
  "s_add_u32 m0, s57, 24576\n\t" \
  "s_nop 0\n\t" \
  "global_load_lds_dwordx4 v236, s[40:41]\n\t" \
  "v_mfma_f32_16x16x32_bf16 v[84:87], v[104:107], v[148:151], v[84:87]\n\t" \
  "s_waitcnt lgkmcnt(12)\n\t" \
  "v_mfma_f32_16x16x32_bf16 v[28:31], v[108:111], v[136:139], v[28:31]\n\t" \
  "v_mfma_f32_16x16x32_bf16 v[48:51], v[108:111], v[140:143], v[48:51]\n\t" \
  "s_mov_b32 s40, 0x1000000\n\t" \
  "Lq%=:\n\t" \
  "ds_read_b32 v13, v241 offset:8\n\t" \
  "s_waitcnt lgkmcnt(0)\n\t" \
  "v_readfirstlane_b32 s41, v13\n\t" \
  "s_sub_u32 s40, s40, 1\n\t" \
  "s_cselect_b32 s41, 1, s41\n\t" \
  "s_cmp_lg_u32 s41, 0\n\t" \
  "s_cbranch_scc1 Lqx%=\n\t" \
  "s_sleep 1\n\t" \
  "s_branch Lq%=\n\t" \
  "Lqx%=:\n\t" \
  "global_load_dwordx4 v[200:203], v14, s[42:43] offset:0 sc1\n\t" \
  "v_mfma_f32_16x16x32_bf16 v[68:71], v[108:111], v[144:147], v[68:71]\n\t" \
  "v_mfma_f32_16x16x32_bf16 v[88:91], v[108:111], v[148:151], v[88:91]\n\t" \
  "v_mfma_f32_16x16x32_bf16 v[32:35], v[112:115], v[136:139], v[32:35]\n\t" \
  "global_load_dwordx4 v[204:207], v14, s[44:45] offset:0 sc1\n\t" \
  "v_mfma_f32_16x16x32_bf16 v[52:55], v[112:115], v[140:143], v[52:55]\n\t" \
  "v_mfma_f32_16x16x32_bf16 v[72:75], v[112:115], v[144:147], v[72:75]\n\t" \
  "v_mfma_f32_16x16x32_bf16 v[92:95], v[112:115], v[148:151], v[92:95]\n\t" \
  "global_load_dwordx4 v[208:211], v14, s[46:47] offset:0 sc1\n\t" \
  "s_waitcnt vmcnt(29)\n\t" \
  "v_mfma_f32_16x16x32_bf16 v[16:19], v[116:119], v[152:155], v[16:19]\n\t" \
  "s_waitcnt vmcnt(16)\n\t" \
  "ds_read_b64 v[96:97], v9 offset:0\n\t" \
  "v_mfma_f32_16x16x32_bf16 v[36:39], v[116:119], v[156:159], v[36:39]\n\t" \
  "ds_read_b64 v[98:99], v10 offset:0\n\t" \
  "v_mfma_f32_16x16x32_bf16 v[56:59], v[116:119], v[160:163], v[56:59]\n\t" \
  "ds_read_b64 v[100:101], v9 offset:2048\n\t" \
  "global_load_dwordx4 v[212:215], v14, s[48:49] offset:0 sc1\n\t" \
  "v_mfma_f32_16x16x32_bf16 v[76:79], v[116:119], v[164:167], v[76:79]\n\t" \
  "ds_read_b64 v[102:103], v10 offset:2048\n\t" \
  "v_mfma_f32_16x16x32_bf16 v[20:23], v[120:123], v[152:155], v[20:23]\n\t" \
  "ds_read_b64 v[104:105], v9 offset:4096\n\t" \
  "v_mfma_f32_16x16x32_bf16 v[40:43], v[120:123], v[156:159], v[40:43]\n\t" \
  "ds_read_b64 v[106:107], v10 offset:4096\n\t" \
  "s_add_u32 s40, s58, 81920\n\t" \
  "s_addc_u32 s41, s59, 0\n\t" \
  "s_add_u32 m0, s57, 25600\n\t" \
  "s_nop 0\n\t" \
  "global_load_lds_dwordx4 v232, s[40:41]\n\t" \
  "v_mfma_f32_16x16x32_bf16 v[60:63], v[120:123], v[160:163], v[60:63]\n\t" \
  "ds_read_b64 v[108:109], v9 offset:6144\n\t" \
  "v_mfma_f32_16x16x32_bf16 v[80:83], v[120:123], v[164:167], v[80:83]\n\t" \
  "ds_read_b64 v[110:111], v10 offset:6144\n\t" \
  "v_mfma_f32_16x16x32_bf16 v[24:27], v[124:127], v[152:155], v[24:27]\n\t" \
  "ds_read_b64 v[112:113], v9 offset:8192\n\t" \
  "global_load_lds_dwordx4 v233, s[40:41] offset:1024\n\t" \
  "v_mfma_f32_16x16x32_bf16 v[44:47], v[124:127], v[156:159], v[44:47]\n\t" \
  "ds_read_b64 v[114:115], v10 offset:8192\n\t" \
  "v_mfma_f32_16x16x32_bf16 v[64:67], v[124:127], v[160:163], v[64:67]\n\t" \
  "v_mfma_f32_16x16x32_bf16 v[84:87], v[124:127], v[164:167], v[84:87]\n\t" \
  "global_load_lds_dwordx4 v234, s[40:41] offset:2048\n\t" \
  "v_mfma_f32_16x16x32_bf16 v[28:31], v[128:131], v[152:155], v[28:31]\n\t" \
  "v_mfma_f32_16x16x32_bf16 v[48:51], v[128:131], v[156:159], v[48:51]\n\t" \
  "v_mfma_f32_16x16x32_bf16 v[68:71], v[128:131], v[160:163], v[68:71]\n\t" \
  "global_load_lds_dwordx4 v235, s[40:41] offset:3072\n\t" \
  "v_mfma_f32_16x16x32_bf16 v[88:91], v[128:131], v[164:167], v[88:91]\n\t" \
  "v_mfma_f32_16x16x32_bf16 v[32:35], v[132:135], v[152:155], v[32:35]\n\t" \
  "v_mfma_f32_16x16x32_bf16 v[52:55], v[132:135], v[156:159], v[52:55]\n\t" \
  "s_add_u32 m0, s57, 29696\n\t" \
  "s_nop 0\n\t" \
  "global_load_lds_dwordx4 v236, s[40:41]\n\t" \
  "v_mfma_f32_16x16x32_bf16 v[72:75], v[132:135], v[160:163], v[72:75]\n\t" \
  "v_mfma_f32_16x16x32_bf16 v[92:95], v[132:135], v[164:167], v[92:95]\n\t" \
  "s_waitcnt vmcnt(21)\n\t" \
  "s_waitcnt lgkmcnt(8)\n\t" \
  "v_mfma_f32_16x16x32_bf16 v[16:19], v[96:99], v[168:171], v[16:19]\n\t" \
  "ds_read_b64 v[116:117], v11 offset:0\n\t" \
  "s_add_u32 s40, s58, 122880\n\t" \
  "s_addc_u32 s41, s59, 0\n\t" \
  "s_add_u32 m0, s57, 10240\n\t" \
  "s_nop 0\n\t" \
  "global_load_lds_dwordx4 v232, s[40:41]\n\t" \
  "s_waitcnt vmcnt(21)\n\t" \
  "v_mfma_f32_16x16x32_bf16 v[36:39], v[96:99], v[172:175], v[36:39]\n\t" \
  "ds_read_b64 v[118:119], v12 offset:0\n\t" \
  "s_waitcnt vmcnt(20)\n\t" \
  "v_mfma_f32_16x16x32_bf16 v[56:59], v[96:99], v[176:179], v[56:59]\n\t" \
  "ds_read_b64 v[120:121], v11 offset:2048\n\t" \
  "s_waitcnt vmcnt(19)\n\t" \
  "v_mfma_f32_16x16x32_bf16 v[76:79], v[96:99], v[180:183], v[76:79]\n\t" \
  "ds_read_b64 v[122:123], v12 offset:2048\n\t" \
  "global_load_lds_dwordx4 v233, s[40:41] offset:1024\n\t" \
  "s_waitcnt lgkmcnt(10)\n\t" \
  "v_mfma_f32_16x16x32_bf16 v[20:23], v[100:103], v[168:171], v[20:23]\n\t" \
  "ds_read_b64 v[124:125], v11 offset:4096\n\t" \
  "v_mfma_f32_16x16x32_bf16 v[40:43], v[100:103], v[172:175], v[40:43]\n\t" \
  "ds_read_b64 v[126:127], v12 offset:4096\n\t" \
  "v_mfma_f32_16x16x32_bf16 v[60:63], v[100:103], v[176:179], v[60:63]\n\t" \
  "ds_read_b64 v[128:129], v11 offset:6144\n\t" \
  "global_load_lds_dwordx4 v234, s[40:41] offset:2048\n\t" \
  "v_mfma_f32_16x16x32_bf16 v[80:83], v[100:103], v[180:183], v[80:83]\n\t" \
  "ds_read_b64 v[130:131], v12 offset:6144\n\t" \
  "s_waitcnt lgkmcnt(12)\n\t" \
  "v_mfma_f32_16x16x32_bf16 v[24:27], v[104:107], v[168:171], v[24:27]\n\t" \
  "ds_read_b64 v[132:133], v11 offset:8192\n\t" \
  "v_mfma_f32_16x16x32_bf16 v[44:47], v[104:107], v[172:175], v[44:47]\n\t" \
  "ds_read_b64 v[134:135], v12 offset:8192\n\t" \
  "global_load_lds_dwordx4 v235, s[40:41] offset:3072\n\t" \
  "v_mfma_f32_16x16x32_bf16 v[64:67], v[104:107], v[176:179], v[64:67]\n\t" \
  "v_mfma_f32_16x16x32_bf16 v[84:87], v[104:107], v[180:183], v[84:87]\n\t" \
  "s_waitcnt lgkmcnt(12)\n\t" \
  "v_mfma_f32_16x16x32_bf16 v[28:31], v[108:111], v[168:171], v[28:31]\n\t" \
  "s_add_u32 m0, s57, 14336\n\t" \
  "s_nop 0\n\t" \
  "global_load_lds_dwordx4 v236, s[40:41]\n\t" \
  "v_mfma_f32_16x16x32_bf16 v[48:51], v[108:111], v[172:175], v[48:51]\n\t" \
  "v_mfma_f32_16x16x32_bf16 v[68:71], v[108:111], v[176:179], v[68:71]\n\t" \
  "v_mfma_f32_16x16x32_bf16 v[88:91], v[108:111], v[180:183], v[88:91]\n\t" \
  "global_load_dwordx4 v[216:219], v14, s[42:43] offset:1024 sc1\n\t" \
  "s_waitcnt lgkmcnt(10)\n\t" \
  "v_mfma_f32_16x16x32_bf16 v[32:35], v[112:115], v[168:171], v[32:35]\n\t" \
  "v_mfma_f32_16x16x32_bf16 v[52:55], v[112:115], v[172:175], v[52:55]\n\t" \
  "v_mfma_f32_16x16x32_bf16 v[72:75], v[112:115], v[176:179], v[72:75]\n\t" \
  "global_load_dwordx4 v[220:223], v14, s[44:45] offset:1024 sc1\n\t" \
  "v_mfma_f32_16x16x32_bf16 v[92:95], v[112:115], v[180:183], v[92:95]\n\t" \
  "s_waitcnt vmcnt(24)\n\t" \
  "s_waitcnt lgkmcnt(8)\n\t" \
  "v_mfma_f32_16x16x32_bf16 v[16:19], v[116:119], v[184:187], v[16:19]\n\t" \
  "ds_read_b64_tr_b16 v[96:97], v4 offset:30720\n\t" \
  "s_waitcnt vmcnt(23)\n\t" \
  "v_mfma_f32_16x16x32_bf16 v[36:39], v[116:119], v[188:191], v[36:39]\n\t" \
  "ds_read_b64_tr_b16 v[98:99], v4 offset:33280\n\t" \
  "global_load_dwordx4 v[224:227], v14, s[46:47] offset:1024 sc1\n\t" \
  "s_waitcnt vmcnt(23)\n\t" \
  "v_mfma_f32_16x16x32_bf16 v[56:59], v[116:119], v[192:195], v[56:59]\n\t" \
  "ds_read_b64_tr_b16 v[100:101], v4 offset:30752\n\t" \
  "s_waitcnt vmcnt(22)\n\t" \
  "v_mfma_f32_16x16x32_bf16 v[76:79], v[116:119], v[196:199], v[76:79]\n\t" \
  "ds_read_b64_tr_b16 v[102:103], v4 offset:33312\n\t" \
  "s_waitcnt lgkmcnt(10)\n\t" \
  "v_mfma_f32_16x16x32_bf16 v[20:23], v[120:123], v[184:187], v[20:23]\n\t" \
  "ds_read_b64_tr_b16 v[104:105], v4 offset:30784\n\t" \
  "global_load_dwordx4 v[228:231], v14, s[48:49] offset:1024 sc1\n\t" \
  "v_mfma_f32_16x16x32_bf16 v[40:43], v[120:123], v[188:191], v[40:43]\n\t" \
  "ds_read_b64_tr_b16 v[106:107], v4 offset:33344\n\t" \
  "v_mfma_f32_16x16x32_bf16 v[60:63], v[120:123], v[192:195], v[60:63]\n\t" \
  "ds_read_b64_tr_b16 v[108:109], v4 offset:30816\n\t" \
  "v_mfma_f32_16x16x32_bf16 v[80:83], v[120:123], v[196:199], v[80:83]\n\t" \
  "ds_read_b64_tr_b16 v[110:111], v4 offset:33376\n\t" \
  "s_add_u32 s40, s58, 163840\n\t" \
  "s_addc_u32 s41, s59, 0\n\t" \
  "s_add_u32 m0, s57, 15360\n\t" \
  "s_nop 0\n\t" \
  "global_load_lds_dwordx4 v232, s[40:41]\n\t" \
  "s_waitcnt lgkmcnt(12)\n\t" \
  "v_mfma_f32_16x16x32_bf16 v[24:27], v[124:127], v[184:187], v[24:27]\n\t" \
  "ds_read_b64_tr_b16 v[112:113], v4 offset:30848\n\t" \
  "v_mfma_f32_16x16x32_bf16 v[44:47], v[124:127], v[188:191], v[44:47]\n\t" \
  "ds_read_b64_tr_b16 v[114:115], v4 offset:33408\n\t" \
  "v_mfma_f32_16x16x32_bf16 v[64:67], v[124:127], v[192:195], v[64:67]\n\t" \
  "global_load_lds_dwordx4 v233, s[40:41] offset:1024\n\t" \
  "v_mfma_f32_16x16x32_bf16 v[84:87], v[124:127], v[196:199], v[84:87]\n\t" \
  "s_waitcnt lgkmcnt(12)\n\t" \
  "v_mfma_f32_16x16x32_bf16 v[28:31], v[128:131], v[184:187], v[28:31]\n\t" \
  "v_mfma_f32_16x16x32_bf16 v[48:51], v[128:131], v[188:191], v[48:51]\n\t" \
  "global_load_lds_dwordx4 v234, s[40:41] offset:2048\n\t" \
  "v_mfma_f32_16x16x32_bf16 v[68:71], v[128:131], v[192:195], v[68:71]\n\t" \
  "v_mfma_f32_16x16x32_bf16 v[88:91], v[128:131], v[196:199], v[88:91]\n\t" \
  "s_waitcnt lgkmcnt(10)\n\t" \
  "v_mfma_f32_16x16x32_bf16 v[32:35], v[132:135], v[184:187], v[32:35]\n\t" \
  "global_load_lds_dwordx4 v235, s[40:41] offset:3072\n\t" \
  "v_mfma_f32_16x16x32_bf16 v[52:55], v[132:135], v[188:191], v[52:55]\n\t" \
  "v_mfma_f32_16x16x32_bf16 v[72:75], v[132:135], v[192:195], v[72:75]\n\t" \
  "v_mul_f32 v16, v237, v16\n\t" \
  "v_mul_f32 v17, v237, v17\n\t" \
  "v_mul_f32 v18, v237, v18\n\t" \
  "v_mul_f32 v19, v237, v19\n\t" \
  "v_mfma_f32_16x16x32_bf16 v[92:95], v[132:135], v[196:199], v[92:95]\n\t" \
  "s_add_u32 m0, s57, 19456\n\t" \
  "s_nop 0\n\t" \
  "global_load_lds_dwordx4 v236, s[40:41]\n\t" \
  "v_mul_f32 v36, v238, v36\n\t" \
  "v_mul_f32 v37, v238, v37\n\t" \
  "v_mul_f32 v38, v238, v38\n\t" \
  "v_mul_f32 v39, v238, v39\n\t" \
  "s_waitcnt vmcnt(22)\n\t" \
  "s_waitcnt lgkmcnt(8)\n\t" \
  "v_mfma_f32_16x16x32_bf16 v[16:19], v[96:99], v[200:203], v[16:19]\n\t" \
  "ds_read_b64_tr_b16 v[116:117], v4 offset:20480\n\t" \
  "v_mul_f32 v56, v239, v56\n\t" \
  "v_mul_f32 v57, v239, v57\n\t" \
  "v_mul_f32 v58, v239, v58\n\t" \
  "v_mul_f32 v59, v239, v59\n\t" \
  "s_waitcnt vmcnt(21)\n\t" \
  "v_mfma_f32_16x16x32_bf16 v[36:39], v[96:99], v[204:207], v[36:39]\n\t" \
  "ds_read_b64_tr_b16 v[118:119], v4 offset:23040\n\t" \
  "v_mul_f32 v76, v240, v76\n\t" \
  "v_mul_f32 v77, v240, v77\n\t" \
  "v_mul_f32 v78, v240, v78\n\t" \
  "v_mul_f32 v79, v240, v79\n\t" \
  "s_waitcnt vmcnt(20)\n\t" \
  "v_mfma_f32_16x16x32_bf16 v[56:59], v[96:99], v[208:211], v[56:59]\n\t" \
  "ds_read_b64_tr_b16 v[120:121], v4 offset:20512\n\t" \
  "global_load_dwordx4 v[136:139], v14, s[42:43] offset:2048 sc1\n\t" \
  "v_mul_f32 v20, v237, v20\n\t" \
  "v_mul_f32 v21, v237, v21\n\t" \
  "v_mul_f32 v22, v237, v22\n\t" \
  "v_mul_f32 v23, v237, v23\n\t" \
  "s_waitcnt vmcnt(20)\n\t" \
  "v_mfma_f32_16x16x32_bf16 v[76:79], v[96:99], v[212:215], v[76:79]\n\t" \
  "ds_read_b64_tr_b16 v[122:123], v4 offset:23072\n\t" \
  "v_mul_f32 v40, v238, v40\n\t" \
  "v_mul_f32 v41, v238, v41\n\t" \
  "v_mul_f32 v42, v238, v42\n\t" \
  "v_mul_f32 v43, v238, v43\n\t" \
  "s_waitcnt lgkmcnt(10)\n\t" \
  "v_mfma_f32_16x16x32_bf16 v[20:23], v[100:103], v[200:203], v[20:23]\n\t" \
  "ds_read_b64_tr_b16 v[124:125], v4 offset:20544\n\t" \
  "v_mul_f32 v60, v239, v60\n\t" \
  "v_mul_f32 v61, v239, v61\n\t" \
  "v_mul_f32 v62, v239, v62\n\t" \
  "v_mul_f32 v63, v239, v63\n\t" \
  "v_mfma_f32_16x16x32_bf16 v[40:43], v[100:103], v[204:207], v[40:43]\n\t" \
  "ds_read_b64_tr_b16 v[126:127], v4 offset:23104\n\t" \
  "global_load_dwordx4 v[140:143], v14, s[44:45] offset:2048 sc1\n\t" \
  "v_mul_f32 v80, v240, v80\n\t" \
  "v_mul_f32 v81, v240, v81\n\t" \
  "v_mul_f32 v82, v240, v82\n\t" \
  "v_mul_f32 v83, v240, v83\n\t" \
  "v_mfma_f32_16x16x32_bf16 v[60:63], v[100:103], v[208:211], v[60:63]\n\t" \
  "ds_read_b64_tr_b16 v[128:129], v4 offset:20576\n\t" \
  "v_mul_f32 v24, v237, v24\n\t" \
  "v_mul_f32 v25, v237, v25\n\t" \
  "v_mul_f32 v26, v237, v26\n\t" \
  "v_mul_f32 v27, v237, v27\n\t" \
  "v_mfma_f32_16x16x32_bf16 v[80:83], v[100:103], v[212:215], v[80:83]\n\t" \
  "ds_read_b64_tr_b16 v[130:131], v4 offset:23136\n\t" \
  "v_mul_f32 v44, v238, v44\n\t" \
  "v_mul_f32 v45, v238, v45\n\t" \
  "v_mul_f32 v46, v238, v46\n\t" \
  "v_mul_f32 v47, v238, v47\n\t" \
  "s_waitcnt lgkmcnt(12)\n\t" \
  "v_mfma_f32_16x16x32_bf16 v[24:27], v[104:107], v[200:203], v[24:27]\n\t" \
  "ds_read_b64_tr_b16 v[132:133], v4 offset:20608\n\t" \
  "global_load_dwordx4 v[144:147], v14, s[46:47] offset:2048 sc1\n\t" \
  "v_mul_f32 v64, v239, v64\n\t" \
  "v_mul_f32 v65, v239, v65\n\t" \
  "v_mul_f32 v66, v239, v66\n\t" \
  "v_mul_f32 v67, v239, v67\n\t" \
  "v_mfma_f32_16x16x32_bf16 v[44:47], v[104:107], v[204:207], v[44:47]\n\t" \
  "ds_read_b64_tr_b16 v[134:135], v4 offset:23168\n\t" \
  "v_mul_f32 v84, v240, v84\n\t" \
  "v_mul_f32 v85, v240, v85\n\t" \
  "v_mul_f32 v86, v240, v86\n\t" \
  "v_mul_f32 v87, v240, v87\n\t" \
  "v_mfma_f32_16x16x32_bf16 v[64:67], v[104:107], v[208:211], v[64:67]\n\t" \
  "v_mul_f32 v28, v237, v28\n\t" \
  "v_mul_f32 v29, v237, v29\n\t" \
  "v_mul_f32 v30, v237, v30\n\t" \
  "v_mul_f32 v31, v237, v31\n\t" \
  "v_mfma_f32_16x16x32_bf16 v[84:87], v[104:107], v[212:215], v[84:87]\n\t" \
  "global_load_dwordx4 v[148:151], v14, s[48:49] offset:2048 sc1\n\t" \
  "v_mul_f32 v48, v238, v48\n\t" \
  "v_mul_f32 v49, v238, v49\n\t" \
  "v_mul_f32 v50, v238, v50\n\t" \
  "v_mul_f32 v51, v238, v51\n\t" \
  "s_waitcnt lgkmcnt(12)\n\t" \
  "v_mfma_f32_16x16x32_bf16 v[28:31], v[108:111], v[200:203], v[28:31]\n\t" \
  "v_mul_f32 v68, v239, v68\n\t" \
  "v_mul_f32 v69, v239, v69\n\t" \
  "v_mul_f32 v70, v239, v70\n\t" \
  "v_mul_f32 v71, v239, v71\n\t" \
  "v_mfma_f32_16x16x32_bf16 v[48:51], v[108:111], v[204:207], v[48:51]\n\t" \
  "v_mul_f32 v88, v240, v88\n\t" \
  "v_mul_f32 v89, v240, v89\n\t" \
  "v_mul_f32 v90, v240, v90\n\t" \
  "v_mul_f32 v91, v240, v91\n\t" \
  "v_mfma_f32_16x16x32_bf16 v[68:71], v[108:111], v[208:211], v[68:71]\n\t" \
  "global_load_dwordx2 v[152:153], v3, s[42:43] offset:0 sc1\n\t" \
  "v_mul_f32 v32, v237, v32\n\t" \
  "v_mul_f32 v33, v237, v33\n\t" \
  "v_mul_f32 v34, v237, v34\n\t" \
  "v_mul_f32 v35, v237, v35\n\t" \
  "v_mfma_f32_16x16x32_bf16 v[88:91], v[108:111], v[212:215], v[88:91]\n\t" \
  "v_mul_f32 v52, v238, v52\n\t" \
  "v_mul_f32 v53, v238, v53\n\t" \
  "v_mul_f32 v54, v238, v54\n\t" \
  "v_mul_f32 v55, v238, v55\n\t" \
  "s_waitcnt lgkmcnt(10)\n\t" \
  "v_mfma_f32_16x16x32_bf16 v[32:35], v[112:115], v[200:203], v[32:35]\n\t" \
  "v_mul_f32 v72, v239, v72\n\t" \
  "v_mul_f32 v73, v239, v73\n\t" \
  "v_mul_f32 v74, v239, v74\n\t" \
  "v_mul_f32 v75, v239, v75\n\t" \
  "v_mfma_f32_16x16x32_bf16 v[52:55], v[112:115], v[204:207], v[52:55]\n\t" \
  "global_load_dwordx2 v[154:155], v3, s[42:43] offset:512 sc1\n\t" \
  "v_mul_f32 v92, v240, v92\n\t" \
  "v_mul_f32 v93, v240, v93\n\t" \
  "v_mul_f32 v94, v240, v94\n\t" \
  "v_mul_f32 v95, v240, v95\n\t" \
  "v_mfma_f32_16x16x32_bf16 v[72:75], v[112:115], v[208:211], v[72:75]\n\t" \
  "v_mfma_f32_16x16x32_bf16 v[92:95], v[112:115], v[212:215], v[92:95]\n\t" \
  "s_waitcnt vmcnt(14)\n\t" \
  "s_waitcnt lgkmcnt(8)\n\t" \
  "v_mfma_f32_16x16x32_bf16 v[16:19], v[116:119], v[216:219], v[16:19]\n\t" \
  "ds_read_b64_tr_b16 v[96:97], v4 offset:28160\n\t" \
  "global_load_dwordx2 v[156:157], v3, s[44:45] offset:0 sc1\n\t" \
  "s_waitcnt vmcnt(14)\n\t" \
  "v_mfma_f32_16x16x32_bf16 v[36:39], v[116:119], v[220:223], v[36:39]\n\t" \
  "ds_read_b64_tr_b16 v[98:99], v4 offset:10240\n\t" \
  "s_waitcnt vmcnt(13)\n\t" \
  "v_mfma_f32_16x16x32_bf16 v[56:59], v[116:119], v[224:227], v[56:59]\n\t" \
  "ds_read_b64_tr_b16 v[100:101], v4 offset:28192\n\t" \
  "s_waitcnt vmcnt(12)\n\t" \
  "v_mfma_f32_16x16x32_bf16 v[76:79], v[116:119], v[228:231], v[76:79]\n\t" \
  "ds_read_b64_tr_b16 v[102:103], v4 offset:10272\n\t" \
  "global_load_dwordx2 v[158:159], v3, s[44:45] offset:512 sc1\n\t" \
  "s_waitcnt lgkmcnt(10)\n\t" \
  "v_mfma_f32_16x16x32_bf16 v[20:23], v[120:123], v[216:219], v[20:23]\n\t" \
  "ds_read_b64_tr_b16 v[104:105], v4 offset:28224\n\t" \
  "v_mfma_f32_16x16x32_bf16 v[40:43], v[120:123], v[220:223], v[40:43]\n\t" \
  "ds_read_b64_tr_b16 v[106:107], v4 offset:10304\n\t" \
  "v_mfma_f32_16x16x32_bf16 v[60:63], v[120:123], v[224:227], v[60:63]\n\t" \
  "ds_read_b64_tr_b16 v[108:109], v4 offset:28256\n\t" \
  "global_load_dwordx2 v[160:161], v3, s[46:47] offset:0 sc1\n\t" \
  "v_mfma_f32_16x16x32_bf16 v[80:83], v[120:123], v[228:231], v[80:83]\n\t" \
  "ds_read_b64_tr_b16 v[110:111], v4 offset:10336\n\t" \
  "s_waitcnt lgkmcnt(12)\n\t" \
  "v_mfma_f32_16x16x32_bf16 v[24:27], v[124:127], v[216:219], v[24:27]\n\t" \
  "ds_read_b64_tr_b16 v[112:113], v4 offset:28288\n\t" \
  "v_mfma_f32_16x16x32_bf16 v[44:47], v[124:127], v[220:223], v[44:47]\n\t" \
  "ds_read_b64_tr_b16 v[114:115], v4 offset:10368\n\t" \
  "global_load_dwordx2 v[162:163], v3, s[46:47] offset:512 sc1\n\t" \
  "v_mfma_f32_16x16x32_bf16 v[64:67], v[124:127], v[224:227], v[64:67]\n\t" \
  "v_mfma_f32_16x16x32_bf16 v[84:87], v[124:127], v[228:231], v[84:87]\n\t" \
  "s_waitcnt lgkmcnt(12)\n\t" \
  "v_mfma_f32_16x16x32_bf16 v[28:31], v[128:131], v[216:219], v[28:31]\n\t" \
  "global_load_dwordx2 v[164:165], v3, s[48:49] offset:0 sc1\n\t" \
  "v_mfma_f32_16x16x32_bf16 v[48:51], v[128:131], v[220:223], v[48:51]\n\t" \
  "v_mfma_f32_16x16x32_bf16 v[68:71], v[128:131], v[224:227], v[68:71]\n\t" \
  "v_mfma_f32_16x16x32_bf16 v[88:91], v[128:131], v[228:231], v[88:91]\n\t" \
  "global_load_dwordx2 v[166:167], v3, s[48:49] offset:512 sc1\n\t" \
  "s_waitcnt lgkmcnt(10)\n\t" \
  "v_mfma_f32_16x16x32_bf16 v[32:35], v[132:135], v[216:219], v[32:35]\n\t" \
  "v_mfma_f32_16x16x32_bf16 v[52:55], v[132:135], v[220:223], v[52:55]\n\t" \
  "v_mfma_f32_16x16x32_bf16 v[72:75], v[132:135], v[224:227], v[72:75]\n\t" \
  "global_load_dwordx4 v[168:171], v14, s[42:43] offset:3072 sc1\n\t" \
  "v_mfma_f32_16x16x32_bf16 v[92:95], v[132:135], v[228:231], v[92:95]\n\t" \
  "s_waitcnt vmcnt(12)\n\t" \
  "s_waitcnt lgkmcnt(8)\n\t" \
  "v_mfma_f32_16x16x32_bf16 v[16:19], v[96:99], v[136:139], v[16:19]\n\t" \
  "ds_read_b64_tr_b16 v[116:117], v4 offset:25600\n\t" \
  "s_waitcnt vmcnt(11)\n\t" \
  "v_mfma_f32_16x16x32_bf16 v[36:39], v[96:99], v[140:143], v[36:39]\n\t" \
  "ds_read_b64_tr_b16 v[118:119], v4 offset:17920\n\t" \
  "global_load_dwordx4 v[172:175], v14, s[44:45] offset:3072 sc1\n\t" \
  "s_waitcnt vmcnt(11)\n\t" \
  "v_mfma_f32_16x16x32_bf16 v[56:59], v[96:99], v[144:147], v[56:59]\n\t" \
  "ds_read_b64_tr_b16 v[120:121], v4 offset:25632\n\t" \
  "s_waitcnt vmcnt(10)\n\t" \
  "v_mfma_f32_16x16x32_bf16 v[76:79], v[96:99], v[148:151], v[76:79]\n\t" \
  "ds_read_b64_tr_b16 v[122:123], v4 offset:17952\n\t" \
  "s_waitcnt lgkmcnt(10)\n\t" \
  "v_mfma_f32_16x16x32_bf16 v[20:23], v[100:103], v[136:139], v[20:23]\n\t" \
  "ds_read_b64_tr_b16 v[124:125], v4 offset:25664\n\t" \
  "global_load_dwordx4 v[176:179], v14, s[46:47] offset:3072 sc1\n\t" \
  "v_mfma_f32_16x16x32_bf16 v[40:43], v[100:103], v[140:143], v[40:43]\n\t" \
  "ds_read_b64_tr_b16 v[126:127], v4 offset:17984\n\t" \
  "v_mfma_f32_16x16x32_bf16 v[60:63], v[100:103], v[144:147], v[60:63]\n\t" \
  "ds_read_b64_tr_b16 v[128:129], v4 offset:25696\n\t" \
  "v_mfma_f32_16x16x32_bf16 v[80:83], v[100:103], v[148:151], v[80:83]\n\t" \
  "ds_read_b64_tr_b16 v[130:131], v4 offset:18016\n\t" \
  "global_load_dwordx4 v[180:183], v14, s[48:49] offset:3072 sc1\n\t" \
  "s_waitcnt lgkmcnt(12)\n\t" \
  "v_mfma_f32_16x16x32_bf16 v[24:27], v[104:107], v[136:139], v[24:27]\n\t" \
  "ds_read_b64_tr_b16 v[132:133], v4 offset:25728\n\t" \
  "v_mfma_f32_16x16x32_bf16 v[44:47], v[104:107], v[140:143], v[44:47]\n\t" \
  "ds_read_b64_tr_b16 v[134:135], v4 offset:18048\n\t" \
  "v_mfma_f32_16x16x32_bf16 v[64:67], v[104:107], v[144:147], v[64:67]\n\t" \
  "v_mfma_f32_16x16x32_bf16 v[84:87], v[104:107], v[148:151], v[84:87]\n\t" \
  "s_waitcnt lgkmcnt(12)\n\t" \
  "v_mfma_f32_16x16x32_bf16 v[28:31], v[108:111], v[136:139], v[28:31]\n\t" \
  "v_mfma_f32_16x16x32_bf16 v[48:51], v[108:111], v[140:143], v[48:51]\n\t" \
  "v_mfma_f32_16x16x32_bf16 v[68:71], v[108:111], v[144:147], v[68:71]\n\t" \
  "v_mfma_f32_16x16x32_bf16 v[88:91], v[108:111], v[148:151], v[88:91]\n\t" \
  "s_waitcnt lgkmcnt(10)\n\t" \
  "v_mfma_f32_16x16x32_bf16 v[32:35], v[112:115], v[136:139], v[32:35]\n\t" \
  "v_mfma_f32_16x16x32_bf16 v[52:55], v[112:115], v[140:143], v[52:55]\n\t" \
  "v_mfma_f32_16x16x32_bf16 v[72:75], v[112:115], v[144:147], v[72:75]\n\t" \
  "v_mfma_f32_16x16x32_bf16 v[92:95], v[112:115], v[148:151], v[92:95]\n\t" \
  "s_waitcnt vmcnt(10)\n\t" \
  "s_waitcnt lgkmcnt(8)\n\t" \
  "v_mfma_f32_16x16x32_bf16 v[16:19], v[116:119], v[152:155], v[16:19]\n\t" \
  "ds_read_b64_tr_b16 v[96:97], v4 offset:12800\n\t" \
  "s_waitcnt vmcnt(8)\n\t" \
  "v_mfma_f32_16x16x32_bf16 v[36:39], v[116:119], v[156:159], v[36:39]\n\t" \
  "ds_read_b64_tr_b16 v[98:99], v4 offset:15360\n\t" \
  "s_waitcnt vmcnt(6)\n\t" \
  "v_mfma_f32_16x16x32_bf16 v[56:59], v[116:119], v[160:163], v[56:59]\n\t" \
  "ds_read_b64_tr_b16 v[100:101], v4 offset:12832\n\t" \
  "s_waitcnt vmcnt(4)\n\t" \
  "v_mfma_f32_16x16x32_bf16 v[76:79], v[116:119], v[164:167], v[76:79]\n\t" \
  "ds_read_b64_tr_b16 v[102:103], v4 offset:15392\n\t" \
  "s_waitcnt lgkmcnt(10)\n\t" \
  "v_mfma_f32_16x16x32_bf16 v[20:23], v[120:123], v[152:155], v[20:23]\n\t" \
  "ds_read_b64_tr_b16 v[104:105], v4 offset:12864\n\t" \
  "v_mfma_f32_16x16x32_bf16 v[40:43], v[120:123], v[156:159], v[40:43]\n\t" \
  "ds_read_b64_tr_b16 v[106:107], v4 offset:15424\n\t" \
  "v_mfma_f32_16x16x32_bf16 v[60:63], v[120:123], v[160:163], v[60:63]\n\t" \
  "ds_read_b64_tr_b16 v[108:109], v4 offset:12896\n\t" \
  "v_mfma_f32_16x16x32_bf16 v[80:83], v[120:123], v[164:167], v[80:83]\n\t" \
  "ds_read_b64_tr_b16 v[110:111], v4 offset:15456\n\t" \
  "s_waitcnt lgkmcnt(12)\n\t" \
  "v_mfma_f32_16x16x32_bf16 v[24:27], v[124:127], v[152:155], v[24:27]\n\t" \
  "ds_read_b64_tr_b16 v[112:113], v4 offset:12928\n\t" \
  "v_mfma_f32_16x16x32_bf16 v[44:47], v[124:127], v[156:159], v[44:47]\n\t" \
  "ds_read_b64_tr_b16 v[114:115], v4 offset:15488\n\t" \
  "v_mfma_f32_16x16x32_bf16 v[64:67], v[124:127], v[160:163], v[64:67]\n\t" \
  "v_mfma_f32_16x16x32_bf16 v[84:87], v[124:127], v[164:167], v[84:87]\n\t" \
  "s_waitcnt lgkmcnt(12)\n\t" \
  "v_mfma_f32_16x16x32_bf16 v[28:31], v[128:131], v[152:155], v[28:31]\n\t" \
  "v_mfma_f32_16x16x32_bf16 v[48:51], v[128:131], v[156:159], v[48:51]\n\t" \
  "v_mfma_f32_16x16x32_bf16 v[68:71], v[128:131], v[160:163], v[68:71]\n\t" \
  "v_mfma_f32_16x16x32_bf16 v[88:91], v[128:131], v[164:167], v[88:91]\n\t" \
  "s_waitcnt lgkmcnt(10)\n\t" \
  "v_mfma_f32_16x16x32_bf16 v[32:35], v[132:135], v[152:155], v[32:35]\n\t" \
  "v_mfma_f32_16x16x32_bf16 v[52:55], v[132:135], v[156:159], v[52:55]\n\t" \
  "v_mfma_f32_16x16x32_bf16 v[72:75], v[132:135], v[160:163], v[72:75]\n\t" \
  "v_mfma_f32_16x16x32_bf16 v[92:95], v[132:135], v[164:167], v[92:95]\n\t" \
  "s_waitcnt vmcnt(2)\n\t" \
  "s_waitcnt lgkmcnt(8)\n\t" \
  "v_mfma_f32_16x16x32_bf16 v[36:39], v[96:99], v[172:175], v[36:39]\n\t" \
  "s_waitcnt lgkmcnt(6)\n\t" \
  "v_mfma_f32_16x16x32_bf16 v[40:43], v[100:103], v[172:175], v[40:43]\n\t" \
  "s_waitcnt lgkmcnt(4)\n\t" \
  "v_mfma_f32_16x16x32_bf16 v[44:47], v[104:107], v[172:175], v[44:47]\n\t" \
  "s_waitcnt lgkmcnt(2)\n\t" \
  "v_mfma_f32_16x16x32_bf16 v[48:51], v[108:111], v[172:175], v[48:51]\n\t" \
  "s_waitcnt lgkmcnt(0)\n\t" \
  "v_mfma_f32_16x16x32_bf16 v[52:55], v[112:115], v[172:175], v[52:55]\n\t" \
  "s_waitcnt vmcnt(1)\n\t" \
  "v_mfma_f32_16x16x32_bf16 v[56:59], v[96:99], v[176:179], v[56:59]\n\t" \
  "v_mfma_f32_16x16x32_bf16 v[60:63], v[100:103], v[176:179], v[60:63]\n\t" \
  "v_mfma_f32_16x16x32_bf16 v[64:67], v[104:107], v[176:179], v[64:67]\n\t" \
  "v_mfma_f32_16x16x32_bf16 v[68:71], v[108:111], v[176:179], v[68:71]\n\t" \
  "ds_write_b128 v15, v[36:39] offset:0\n\t" \
  "v_mfma_f32_16x16x32_bf16 v[72:75], v[112:115], v[176:179], v[72:75]\n\t" \
  "ds_write_b128 v15, v[40:43] offset:1024\n\t" \
  "s_waitcnt vmcnt(0)\n\t" \
  "v_mfma_f32_16x16x32_bf16 v[76:79], v[96:99], v[180:183], v[76:79]\n\t" \
  "ds_write_b128 v15, v[44:47] offset:2048\n\t" \
  "v_mfma_f32_16x16x32_bf16 v[80:83], v[100:103], v[180:183], v[80:83]\n\t" \
  "ds_write_b128 v15, v[48:51] offset:3072\n\t" \
  "v_mfma_f32_16x16x32_bf16 v[84:87], v[104:107], v[180:183], v[84:87]\n\t" \
  "ds_write_b128 v15, v[52:55] offset:4096\n\t" \
  "v_mfma_f32_16x16x32_bf16 v[88:91], v[108:111], v[180:183], v[88:91]\n\t" \
  "ds_write_b128 v15, v[56:59] offset:5120\n\t" \
  "v_mfma_f32_16x16x32_bf16 v[92:95], v[112:115], v[180:183], v[92:95]\n\t" \
  "ds_write_b128 v15, v[60:63] offset:6144\n\t" \
  "v_mfma_f32_16x16x32_bf16 v[16:19], v[96:99], v[168:171], v[16:19]\n\t" \
  "ds_write_b128 v15, v[64:67] offset:7168\n\t" \
  "v_mfma_f32_16x16x32_bf16 v[20:23], v[100:103], v[168:171], v[20:23]\n\t" \
  "ds_write_b128 v15, v[68:71] offset:8192\n\t" \
  "v_mfma_f32_16x16x32_bf16 v[24:27], v[104:107], v[168:171], v[24:27]\n\t" \
  "ds_write_b128 v15, v[72:75] offset:9216\n\t" \
  "v_mfma_f32_16x16x32_bf16 v[28:31], v[108:111], v[168:171], v[28:31]\n\t" \
  "ds_write_b128 v15, v[76:79] offset:10240\n\t" \
  "v_mfma_f32_16x16x32_bf16 v[32:35], v[112:115], v[168:171], v[32:35]\n\t" \
  "ds_write_b128 v15, v[80:83] offset:11264\n\t" \
  "s_nop 2\n\t" \
  "ds_write_b128 v15, v[84:87] offset:12288\n\t" \
  "ds_write_b128 v15, v[88:91] offset:13312\n\t" \
  "ds_write_b128 v15, v[92:95] offset:14336\n\t" \
  "s_waitcnt lgkmcnt(0)\n\t" \
  "s_barrier\n\t" \
  "s_nop 7\n\t" \
  ""
#define S2_CLOB "v36", "v37", "v38", "v39", "v40", "v41", "v42", "v43", "v44", "v45", "v46", "v47", "v48", "v49", "v50", "v51", "v52", "v53", "v54", "v55", "v56", "v57", "v58", "v59", "v60", "v61", "v62", "v63", "v64", "v65", "v66", "v67", "v68", "v69", "v70", "v71", "v72", "v73", "v74", "v75", "v76", "v77", "v78", "v79", "v80", "v81", "v82", "v83", "v84", "v85", "v86", "v87", "v88", "v89", "v90", "v91", "v92", "v93", "v94", "v95", "v96", "v97", "v98", "v99", "v100", "v101", "v102", "v103", "v104", "v105", "v106", "v107", "v108", "v109", "v110", "v111", "v112", "v113", "v114", "v115", "v116", "v117", "v118", "v119", "v120", "v121", "v122", "v123", "v124", "v125", "v126", "v127", "v128", "v129", "v130", "v131", "v132", "v133", "v134", "v135", "v136", "v137", "v138", "v139", "v140", "v141", "v142", "v143", "v144", "v145", "v146", "v147", "v148", "v149", "v150", "v151", "v152", "v153", "v154", "v155", "v156", "v157", "v158", "v159", "v160", "v161", "v162", "v163", "v164", "v165", "v166", "v167", "v168", "v169", "v170", "v171", "v172", "v173", "v174", "v175", "v176", "v177", "v178", "v179", "v180", "v181", "v182", "v183", "v184", "v185", "v186", "v187", "v188", "v189", "v190", "v191", "v192", "v193", "v194", "v195", "v196", "v197", "v198", "v199", "v200", "v201", "v202", "v203", "v204", "v205", "v206", "v207", "v208", "v209", "v210", "v211", "v212", "v213", "v214", "v215", "v216", "v217", "v218", "v219", "v220", "v221", "v222", "v223", "v224", "v225", "v226", "v227", "v228", "v229", "v230", "v231"
#define DFT_CLOB "v16", "v17", "v18", "v19", "v20", "v21", "v22", "v23", "v24", "v25", "v26", "v27", "v28", "v29", "v30", "v31", "v32", "v33", "v34", "v35", "v36", "v37", "v38", "v39", "v40", "v41", "v42", "v43", "v44", "v45", "v46", "v47", "v48", "v49", "v50", "v51", "v52", "v53", "v54", "v55", "v56", "v57", "v58", "v59", "v60", "v61", "v62", "v63", "v64", "v65", "v66", "v67", "v68", "v69", "v70", "v71", "v72", "v73", "v74", "v75", "v76", "v77", "v78", "v79", "v80", "v81", "v82", "v83", "v84", "v85", "v86", "v87", "v88", "v89", "v90", "v91", "v92", "v93", "v94", "v95", "v96", "v97", "v98", "v99", "v100", "v101", "v102", "v103", "v104", "v105", "v106", "v107", "v108", "v109", "v110", "v111", "v112", "v113", "v114", "v115", "v116", "v117", "v118", "v119", "v120", "v121", "v122", "v123", "v124", "v125", "v126", "v127", "v128", "v129", "v130", "v131", "v132", "v133", "v134", "v135", "v136", "v137", "v138", "v139", "v140", "v141", "v142", "v143"
#define QKV_CLOB "v22", "v23", "v24", "v25", "v26", "v27", "v28", "v29", "v30", "v31", "v32", "v33", "v34", "v35", "v36", "v37", "v38", "v39", "v40", "v41", "v42", "v43", "v44", "v45", "v46", "v47", "v48", "v49", "v50", "v51", "v52", "v53", "v54", "v55", "v56", "v57", "v58", "v59", "v60", "v61", "v62", "v63", "v64", "v65"
#define SUB4(v, i) __builtin_shufflevector(v, v, 4 * (i), 4 * (i) + 1, 4 * (i) + 2, 4 * (i) + 3)

#define STAMP(id)
#define RSTAMP_DECL
#define RSTAMP(i)
#define RSTAMP_FLUSH(n)

DEVI const char* qflag(const char* ws, int mb, int lane) {
  return ws + WS_FLAG + (32 + (lane < 8 ? (mb * 64) / 176 : (mb * 64 + 63) / 176)) * 128 + (lane & 7) * 8;
}
DEVI f32x4 stage2_gate(const char* ws, int mb, int lw, int lane, u64 tag) {
  const char* gp = ws + WS_GATE + (mb * 64 + (lane & 15)) * 8;
  u64 gr[4];
#pragma nounroll
  for (int i = 0; i < SPIN_CAP; i++) {
    bool ok = true;
#pragma unroll
    for (int mt = 0; mt < 4; mt++) gr[mt] = ldu64(gp + ((lw + mt) & 3) * 128);
#pragma unroll
    for (int mt = 0; mt < 4; mt++) ok = ok && (u32)(gr[mt] >> 32) == (u32)tag;
    if (__all(ok)) break;
    __builtin_amdgcn_s_sleep(1);
  }
  const u32 sg = lw < 2 ? 0u : 0x80000000u;
  return f32x4{__uint_as_float((u32)gr[0] ^ sg), __uint_as_float((u32)gr[1] ^ sg), __uint_as_float((u32)gr[2] ^ sg), __uint_as_float((u32)gr[3] ^ sg)};
}
DEVI const char* s2_wb(const char* Wr, const char* Wi, int nb, int lw) {
  return (lw < 2 ? Wr : Wi) + nb * 80 * 1280 + (lw & 1) * 640; }
DEVI u32 s2_vw(int i, int lane) { return (8 * i + (lane >> 3)) * 1280 + 16 * ((lane & 7) ^ ((4 * i + (lane >> 4)) & 7)) - i * 1024; }
DEVI int s2_rot(int mb, int nb) { return (nb + (mb & 3)) % 5; }
DEVI void stage2_w0(const char* Wr, const char* Wi, int mb, int nb, int lw, int lane) {
  const char* sb = s2_wb(Wr, Wi, nb, lw) + s2_rot(mb, nb) * 128;
  const u32 d = S2_BASE + lw * S2_REG + 10240;
  const u32 v0 = s2_vw(0, lane), v1 = s2_vw(1, lane), v2 = s2_vw(2, lane), v3 = s2_vw(3, lane);
  dma4(sb, v0, v1, v2, v3, d);
  dma4(sb + 32 * 1280, v0, v1, v2, v3, d + 4096);
  dma2(sb + 64 * 1280, v0, v1, d + 8192);
}

DEVI void stage2(f32x4 gs, const char* Wr, const char* Wi, const char* wo, char* ws, char* out, int mb, int nb, int lw, int lane) {
  const char* ws_ = ws; (void)ws_;
  const int g = lane >> 4, e = lane & 15;
  const u32 R = S2_BASE + lw * S2_REG;
  RSTAMP_DECL;
  RSTAMP(0);
  const char* const wb = s2_wb(Wr, Wi, nb, lw);
  const int rot = s2_rot(mb, nb);
  const u32 vw[4] = {s2_vw(0, lane), s2_vw(1, lane), s2_vw(2, lane), s2_vw(3, lane)};
  const float g0 = gs[0], g1 = gs[1], g2 = gs[2], g3 = gs[3];
  u32 vo[5], so[5];
#pragma unroll
  for (int i = 0; i < 5; i++) { const int n = 64 * i + lane, r = (n * 6554) >> 16; vo[i] = r * 1280 + (n - r * 10) * 16 - (i & 3) * 1024; }
#pragma unroll
  for (int i = 0; i < 5; i++) so[i] = (i + rot >= 5 ? i + rot - 5 : i + rot) * 2048;
  const char* Ab[4];
#pragma unroll
  for (int mt = 0; mt < 4; mt++) Ab[mt] = ws + (size_t)(mb * 4 + ((lw + mt) & 3)) * 61440 + 10 * lw * 1024;
  const u32 va = lane * 16, wq = R + e * 128 + 8 * (g & 1);
  f32x16 a0; f32x4 a1;
  RSTAMP(1);
  asm volatile(S2_ALL
               : "={v[16:31]}"(a0), "={v[32:35]}"(a1)
               : "{v2}"(va), "{v3}"(lane * 8 + (56 - 9 * lw) * 1024), "{v4}"(R + (4 * g + (e >> 2)) * 160 + 8 * (lane & 3)),
                 "{v5}"(vw[0]), "{v6}"(vw[1]), "{v7}"(vw[2]), "{v8}"(vw[3]),
                 "{v9}"(wq + 16 * ((g >> 1) ^ (e >> 1))), "{v10}"(wq + 16 * ((2 + (g >> 1)) ^ (e >> 1))),
                 "{v11}"(wq + 16 * ((4 + (g >> 1)) ^ (e >> 1))), "{v12}"(wq + 16 * ((6 + (g >> 1)) ^ (e >> 1))),
                 "{v14}"(va + (40 - 6 * lw) * 1024), "{v15}"(R + va),
                 "{v232}"(vo[0]), "{v233}"(vo[1]), "{v234}"(vo[2]), "{v235}"(vo[3]), "{v236}"(vo[4]),
                 "{v237}"(g0), "{v238}"(g1), "{v239}"(g2), "{v240}"(g3), "{v241}"(L_CNT),
                 "{s[42:43]}"(Ab[0]), "{s[44:45]}"(Ab[1]), "{s[46:47]}"(Ab[2]), "{s[48:49]}"(Ab[3]), "{s[50:51]}"(wb),
                 "{s52}"(so[0]), "{s53}"(so[1]), "{s54}"(so[2]), "{s55}"(so[3]), "{s56}"(so[4]), "{s57}"(R),
"{s[58:59]}"(wo + lw * 160 * 1280 + nb * 160)
               : S2_CLOB, "v13", "s40", "s41", "scc", "memory");
  RSTAMP(2);
  f32x4 s[5] = {SUB4(a0, 0), SUB4(a0, 1), SUB4(a0, 2), SUB4(a0, 3), a1};
#pragma unroll
  for (int d = 1; d < 4; d++)
#pragma unroll
    for (int c = 0; c < 5; c++) s[c] += *(LDS3 f32x4*)lp(S2_BASE + ((lw - d) & 3) * S2_REG + ((d - 1) * 5 + c) * 1024 + va);
  char* const o = out + (size_t)(mb * 64 + lw * 16 + e) * 1280 + nb * 160;
#pragma unroll
  for (int c = 0; c < 4; c += 2) {
    auto x0 = __builtin_amdgcn_permlane16_swap(pk2(s[c][0], s[c][1]), pk2(s[c + 1][0], s[c + 1][1]), false, false);
    auto x1 = __builtin_amdgcn_permlane16_swap(pk2(s[c][2], s[c][3]), pk2(s[c + 1][2], s[c + 1][3]), false, false);
    *(u32x4*)(o + (c + (g & 1)) * 32 + (g >> 1) * 16) = u32x4{x0[0], x1[0], x0[1], x1[1]};
  }
  *(u32x2*)(o + 128 + g * 8) = u32x2{pk2(s[4][0], s[4][1]), pk2(s[4][2], s[4][3])};
}

DEVI void dft_consumer(char* ws, int mb, int nblk, int w, int lane, u64 tag) {
  const char* ws_ = ws; (void)ws_;
  const int wt = w & 1, wd = w >> 1, g = lane >> 4, e = lane & 15;
  u32 ca0 = (wt * 64 + e) * 128 + 16 * (g ^ (e >> 1)), ca1 = ca0 ^ 64;
  const int q = e >> 2, F = q | ((g & 1) << 2);
  u32 xb[4];
#pragma unroll
  for (int c = 0; c < 4; c++) xb[c] = 65536 + (8 * g + q) * 256 + 8 * (lane & 3) + 32 * ((4 * wd + c) ^ F);
  const u32 so = lane * 16 + (mb * 240 + wt * 20 + nblk * 4 + wd * 2) * 1024;
  u32 cnt = 16;
  asm volatile(DFT_LOOP
               : "+{v8}"(ca0), "+{v9}"(ca1), "+{v10}"(xb[0]), "+{v11}"(xb[1]), "+{v12}"(xb[2]), "+{v13}"(xb[3]), [cnt] "+s"(cnt)
               : "{v144}"(so), "{v145}"(so + 61440), "{v146}"(so + 2 * 61440), "{v147}"(so + 3 * 61440), [ab] "s"(ws)
               : DFT_CLOB, "scc", "memory");
  if (w == 0) STAMP(2);
  publish(ws + WS_FLAG + mb * 128 + nblk * 8, tag, lane);
  if (w == 0) STAMP(3);
}

DEVI f32x4 dft_loader(const char* Cr, const char* Ci, const char* x, const char* Wr, const char* Wi, const char* ws, int xcd, int ml, int nblk, int lw, int lane) {
  const int mb = 4 * xcd + ml;
  const char* Cb = (lw >= 2 ? Ci : Cr) + (size_t)(mb * 64 + (lw & 1) * 32) * 4096;
  const char* Xb = x + nblk * 256 + lw * 16 * 1280;
  u32 vA[4], vB[4];
#pragma unroll
  for (int i = 0; i < 4; i++) {
    vA[i] = (8 * i + (lane >> 3)) * 4096 + 16 * ((lane & 7) ^ ((4 * i + (lane >> 4)) & 7)) - i * 1024;
    vB[i] = (4 * i + (lane >> 4)) * 1280 + 16 * ((lane & 15) ^ (((lane >> 4) | ((i >> 1) << 2)) << 1)) - i * 1024;
  }
  const int phi = (ml + nblk) & 3;
  auto issue = [&](int i) {
    const int ks = (((i & ~3) | ((i + phi) & 3)) + 4 * xcd) & 31;
    const u32 dst = (i & 3) * 16384 + lw * 4096;
    dma4(Cb + ks * 128, vA[0], vA[1], vA[2], vA[3], dst);
    dma4(Xb + ks * 64 * 1280, vB[0], vB[1], vB[2], vB[3], dst + 65536);
  };
  issue(0); issue(1);
  const u64 tag = read_tag(ws);
  waitv<8>(); bar();
  issue(2);
  waitv<8>(); bar();
  issue(3); issue(4);
  for (int t = 1; t < 28; t++) { waitv<16>(); bar(); issue(t + 4); }
  waitv<16>(); bar();
  waitv<8>(); bar();
  const f32x4 gs = stage2_gate(ws, mb, lw, lane, tag);
  waitv<0>(); bar();
  stage2_w0(Wr, Wi, mb, nblk, lw, lane);
  bar();
  bar();
  return gs;
}

#define NTOUCH 9
DEVI void touchw(int tr, const char* Wr, const char* Wi, const char* wo, int hg, int rb, int lw, int lane) {
  if (lw < 3) {
    const int mat = tr / 3, k = tr - 3 * mat + hg % 3;
    const int n = min(((k >= 3 ? k - 3 : k) * 36 + rb * 3 + lw) * 64 + lane, 6399);
    touch(mat == 0 ? Wr : mat == 1 ? Wi : wo, n * 128, L_CNT + 64);
  }
}
DEVI void touchm(const char* Wr, const char* Wi, const char* wo, int id, int lane) {
  const int n = min(id * 50 + lane, 19199), mat = n / 6400;
  const u64 p = (mat == 0 ? (u64)Wr : mat == 1 ? (u64)Wi : (u64)wo) + (u32)((n - mat * 6400) * 128);
  asm volatile("s_mov_b32 m0, %1\n\ts_nop 0\n\tglobal_load_lds_dword %0, off" ::"v"(p), "s"(L_CNT + 64) : "memory");
}
DEVI f32x4 qkv_loader(int mb, int nb, const char* x, const char* wqkv, const char* Wr, const char* Wi, const char* wo, const char* ws, int hg, int rb, int lw, int lane) {
  const int R0 = 176 * rb - 16;
  u32 vA[6], vB[4];
#pragma unroll
  for (int i = 0; i < 6; i++) {
    const int gr = min(max(R0 + 48 * lw + 8 * i + (lane >> 3), 0), 2047);
    vA[i] = gr * 1280 + 16 * ((lane & 7) ^ ((4 * i + (lane >> 4)) & 7)) + 3072 - (i % 3) * 1024;
  }
#pragma unroll
  for (int i = 0; i < 4; i++) {
    const int row = 8 * lw + 2 * i + (lane >> 5);
    int ch = (lane & 31) ^ (((row & 3) | (((row >> 3) & 1) << 2)) << 1);
    if (ch > 29) ch -= 2;
    const int seg = (ch * 26) >> 8;
    vB[i] = row * 3840 + seg * 1120 + ch * 16 - i * 1024;
  }
  const char* const xb = x - 3072;
  const char* const wb = wqkv + hg * 160;
  const int rot = rb >= 10 ? rb - 10 : rb;
  auto kst = [&](int j) { const int k = j + rot; return k >= 10 ? k - 10 : k; };
  auto issueA = [&](int j, int aslot) {
    const char* sb = xb + kst(j) * 128;
    const u32 dst = QA0 + aslot * QA_SLOT + lw * 6144;
    dma3(sb, vA[0], vA[1], vA[2], dst); dma3(sb, vA[3], vA[4], vA[5], dst + 3072);
  };
  auto issueB = [&](int s) { dma4(wb + (kst(s >> 1) * 64 + (s & 1) * 32) * 3840, vB[0], vB[1], vB[2], vB[3], (s & 3) * QB_SLOT + lw * 4096); };
  issueA(0, 0); issueB(0); issueB(1); issueA(1, 1);
  const u64 tag = read_tag(ws);
  waitv<10>(); bar();
  issueB(2); issueA(2, 2);
  waitv<16>(); bar();
  issueB(3);
  int as = 0;
#pragma nounroll
  for (int s = 1; s < 15; s += 2) {
    waitv<10>(); bar(); issueB(s + 3); issueA((s + 5) >> 1, as);
    waitv<10>(); bar(); issueB(s + 4);
    if (s == 5) touchm(Wr, Wi, wo, (rb * 8 + hg) * 4 + lw, lane);
    as = (as == 2) ? 0 : as + 1;
  }
  waitv<10>(); bar(); issueB(18);
  waitv<4>(); bar(); issueB(19);
  waitv<4>(); bar();
  touchw(0, Wr, Wi, wo, hg, rb, lw, lane);
  if (lw < 3) waitv<1>(); else
  waitv<0>();
  bar();
  waitv<0>(); touchw(1, Wr, Wi, wo, hg, rb, lw, lane);
  bar();
  waitv<0>(); touchw(2, Wr, Wi, wo, hg, rb, lw, lane);
  bar();
#pragma nounroll
  for (int tr = 3; tr < NTOUCH; tr++) { waitv<0>(); touchw(tr, Wr, Wi, wo, hg, rb, lw, lane); }
  waitv<0>();
  const f32x4 gs = stage2_gate(ws, mb, lw, lane, tag);
  bar();
  stage2_w0(Wr, Wi, mb, nb, lw, lane);
  return gs;
}

DEVI void qkv_consumer(const char* x, const char* gw, char* ws, int hg, int rb, int w, int lane, u64 tag) {
  const char* ws_ = ws; (void)ws_;
  if (w < 3) {
    const int i = 8 * w + (lane >> 3), t = 176 * rb + 22 * hg + i;
    const char* xr = x + min(t, 2047) * 1280 + (lane & 7) * 16;
    float s = 0.f;
#pragma unroll
    for (int c = 0; c < 10; c++) {
      const u32x4 a = *(const u32x4*)(xr + c * 128), b = *(const u32x4*)(gw + (lane & 7) * 16 + c * 128);
#pragma unroll
      for (int k = 0; k < 4; k++) { const u32 p = a[k], q = b[k]; s = __builtin_amdgcn_fdot2_f32_bf16(__builtin_bit_cast(bf16x2, p), __builtin_bit_cast(bf16x2, q), s, false); }
    }
    s += __shfl_xor(s, 1); s += __shfl_xor(s, 2); s += __shfl_xor(s, 4);
    if ((lane & 7) == 0 && i < 22 && t < 2048) stu64(ws + WS_GATE + t * 8, ((u64)(u32)tag << 32) | __float_as_uint(1.f / (1.f + __expf(-s))));
  }
  const int g = lane >> 4, e = lane & 15;
  u32 xa = QA0 + (48 * w + e) * 128 + 16 * (g ^ (e >> 1));
  const u32 xbase = xa;
  const int q = e >> 2, F = q | ((g & 1) << 2);
  u32 wb[8];
#pragma unroll
  for (int c = 0; c < 8; c++) wb[c] = (8 * g + q) * 512 + 8 * (lane & 3) + 32 * (c ^ F);
  f32x32 a0, a1, a2, a3, a4; f32x16 a5; f32x4 a6;
  u32 cnt = 10, ao = 0;
  asm volatile(QKV_LOOP
               : "={v[66:97]}"(a0), "={v[98:129]}"(a1), "={v[130:161]}"(a2), "={v[162:193]}"(a3), "={v[194:225]}"(a4), "={v[226:241]}"(a5), "={v[242:245]}"(a6),
                 "+{v12}"(wb[0]), "+{v13}"(wb[1]), "+{v14}"(wb[2]), "+{v15}"(wb[3]), "+{v16}"(wb[4]), "+{v17}"(wb[5]), "+{v18}"(wb[6]), "+{v19}"(wb[7]),
                 "+{v20}"(xa), [cnt] "+s"(cnt), [ao] "+s"(ao)
               : "{v21}"(xbase)
               : QKV_CLOB, "scc", "memory");
  const f32x4 acc[3][15] = {
      {SUB4(a0, 0), SUB4(a0, 1), SUB4(a0, 2), SUB4(a0, 3), SUB4(a0, 4), SUB4(a0, 5), SUB4(a0, 6), SUB4(a0, 7), SUB4(a1, 0), SUB4(a1, 1), SUB4(a1, 2), SUB4(a1, 3), SUB4(a1, 4), SUB4(a1, 5), SUB4(a1, 6)},
      {SUB4(a1, 7), SUB4(a2, 0), SUB4(a2, 1), SUB4(a2, 2), SUB4(a2, 3), SUB4(a2, 4), SUB4(a2, 5), SUB4(a2, 6), SUB4(a2, 7), SUB4(a3, 0), SUB4(a3, 1), SUB4(a3, 2), SUB4(a3, 3), SUB4(a3, 4), SUB4(a3, 5)},
      {SUB4(a3, 6), SUB4(a3, 7), SUB4(a4, 0), SUB4(a4, 1), SUB4(a4, 2), SUB4(a4, 3), SUB4(a4, 4), SUB4(a4, 5), SUB4(a4, 6), SUB4(a4, 7), SUB4(a5, 0), SUB4(a5, 1), SUB4(a5, 2), SUB4(a5, 3), a6}};
  const char* gp = ws + WS_GATE;
  u32 go[3];
  u64 gr[3];
#pragma unroll
  for (int m = 0; m < 3; m++) { go[m] = min(max(176 * rb + 48 * w + 16 * m - 16 + e, 0), 2047) * 8; gr[m] = ldu64(gp + go[m]); }
  if (w == 0) STAMP(2);
  u32x10 Kv[4];
  u32x8 Vv[5];
#pragma unroll
  for (int m = 0; m < 3; m++)
#pragma unroll
    for (int c = 0; c < 5; c++) {
      Kv[m + 1][2 * c] = pk2(acc[m][5 + c][0], acc[m][5 + c][1]); Kv[m + 1][2 * c + 1] = pk2(acc[m][5 + c][2], acc[m][5 + c][3]);
      Vv[c][2 * m + 2] = pk2(acc[m][10 + c][0], acc[m][10 + c][1]); Vv[c][2 * m + 3] = pk2(acc[m][10 + c][2], acc[m][10 + c][3]);
    }
  if (w < 3) {
#pragma unroll
    for (int c = 0; c < 5; c++) *(LDS3 u32x4*)lp(L_HALO + (w * 5 + c) * 1024 + lane * 16) = u32x4{Kv[3][2 * c], Kv[3][2 * c + 1], Vv[c][6], Vv[c][7]};
  }
  waitl0(); bar();
  {
    const int wp = (w > 0) ? w - 1 : 0;
#pragma unroll
    for (int c = 0; c < 5; c++) {
      const u32x4 h = *(LDS3 u32x4*)lp(L_HALO + (wp * 5 + c) * 1024 + lane * 16);
      Kv[0][2 * c] = h[0]; Kv[0][2 * c + 1] = h[1]; Vv[c][0] = h[2]; Vv[c][1] = h[3];
    }
  }
#pragma nounroll
  for (int i = 0; i < SPIN_CAP; i++) {
    bool ok = true;
#pragma unroll
    for (int m = 0; m < 3; m++) ok = ok && (u32)(gr[m] >> 32) == (u32)tag;
    if (__all(ok)) break;
    __builtin_amdgcn_s_sleep(1);
#pragma unroll
    for (int m = 0; m < 3; m++) gr[m] = ldu64(gp + go[m]);
  }
  float omg[3];
#pragma unroll
  for (int m = 0; m < 3; m++) { omg[m] = 1.f - __uint_as_float((u32)gr[m]); asm volatile("" : "+v"(omg[m])); }
#pragma unroll
  for (int m = 0; m < 4; m++) asm("" : "+v"(Kv[m]));
#pragma unroll
  for (int c = 0; c < 5; c++) asm("" : "+v"(Vv[c]));
  if (w == 0) STAMP(3);
  const bool par = e & 1;
  const float NEG = -1.0e30f, cs = 0.22360679774997896f * 1.4426950408889634f;
  f32x4 cmP, cmC;
#pragma unroll
  for (int r = 0; r < 4; r++) { cmP[r] = (4 * g + r >= e + 2) ? 0.f : NEG; cmC[r] = (4 * g + r <= e) ? 0.f : NEG; }
  const u32 selL = par ? 0x01000c0cu : 0x0c0c0100u, selH = par ? 0x03020c0cu : 0x0c0c0302u;
  const auto rs = __builtin_amdgcn_make_buffer_rsrc(ws, 0, A2_BYTES, 0x00020000);
#pragma unroll
  for (int m = 0; m < 3; m++) {
    const int tq = 11 * rb + 3 * w + m - 1;
    if ((m == 0 && w == 0) || tq >= 128) continue;
    const f32x4 cp = tq > 0 ? cmP : f32x4{NEG, NEG, NEG, NEG};
    u32 qp[10];
#pragma unroll
    for (int c = 0; c < 5; c++) { qp[2 * c] = pk2(acc[m][c][0], acc[m][c][1]); qp[2 * c + 1] = pk2(acc[m][c][2], acc[m][c][3]); }
    float s[4][4], mx[4];
#define KW(v, h) b8(__builtin_shufflevector(v, v, 2 * (h), 2 * (h) + 1, 2 * (h) + 2, 2 * (h) + 3))
#define HEAD(h) { \
      const bf16x8 Q = b8(u32x4{g >= h ? qp[2 * h] : 0u, g >= h ? qp[2 * h + 1] : 0u, g <= h ? qp[2 * h + 2] : 0u, g <= h ? qp[2 * h + 3] : 0u}); \
      const f32x4 Sp = mfma(KW(Kv[m], h), Q, cp), Sc = mfma(KW(Kv[m + 1], h), Q, cmC); \
      s[h][0] = par ? Sp[1] : Sp[0]; s[h][1] = par ? Sp[3] : Sp[2]; s[h][2] = par ? Sc[1] : Sc[0]; s[h][3] = par ? Sc[3] : Sc[2]; \
      mx[h] = fmax3n(s[h][0], s[h][1], fmax2(s[h][2], s[h][3])); }
    HEAD(0) HEAD(1) HEAD(2) HEAD(3)
    float bm[4];
    {
      const f32x2 u = sw16(mx[0], mx[1]), v = sw16(mx[2], mx[3]);
      const f32x2 t = sw32(fmax2n(u[0], u[1]), fmax2n(v[0], v[1]));
      const float W = -cs * fmax2(t[0], t[1]);
      const f32x2 y = sw32(W, W), a = sw16(y[0], y[0]), b = sw16(y[1], y[1]);
      bm[0] = a[0]; bm[1] = a[1]; bm[2] = b[0]; bm[3] = b[1];
    }
    float sm[4];
    f32x4 lo[4], hi[4];
#define VW(c) b8(m == 0 ? __builtin_shufflevector(Vv[c], Vv[c], 0, 1, 2, 3) : m == 1 ? __builtin_shufflevector(Vv[c], Vv[c], 2, 3, 4, 5) : __builtin_shufflevector(Vv[c], Vv[c], 4, 5, 6, 7))
#pragma unroll
    for (int h = 0; h < 4; h++) {
      float p[4];
#pragma unroll
      for (int i = 0; i < 4; i++) p[i] = __builtin_amdgcn_exp2f(__builtin_fmaf(s[h][i], cs, bm[h]));
      sm[h] = (p[0] + p[1]) + (p[2] + p[3]);
      const u32 r01 = pk2(p[0], p[1]), r23 = pk2(p[2], p[3]);
      const bf16x8 P = b8(u32x4{__builtin_amdgcn_perm(0u, r01, selL), __builtin_amdgcn_perm(0u, r01, selH), __builtin_amdgcn_perm(0u, r23, selL), __builtin_amdgcn_perm(0u, r23, selH)});
      lo[h] = mfma(VW(h), P, f32x4{0, 0, 0, 0});
      hi[h] = mfma(VW(h + 1), P, f32x4{0, 0, 0, 0});
    }
    float iv[4];
    {
      const f32x2 u = sw16(sm[0], sm[1]), v = sw16(sm[2], sm[3]);
      const f32x2 t = sw32(u[0] + u[1], v[0] + v[1]);
      const float W = omg[m] * __builtin_amdgcn_rcpf(t[0] + t[1]);
      const f32x2 y = sw32(W, W), a = sw16(y[0], y[0]), b = sw16(y[1], y[1]);
      iv[0] = a[0]; iv[1] = a[1]; iv[2] = b[0]; iv[3] = b[1];
    }
    const f32x4 o0 = lo[0] * iv[0], o4 = hi[3] * iv[3];
    const f32x4 o1 = (g >= 1 ? lo[1] : hi[0]) * (g >= 1 ? iv[1] : iv[0]);
    const f32x4 o2 = (g >= 2 ? lo[2] : hi[1]) * (g >= 2 ? iv[2] : iv[1]);
    const f32x4 o3 = (g >= 3 ? lo[3] : hi[2]) * (g >= 3 ? iv[3] : iv[2]);
    const int base = tq * 61440;
    __builtin_amdgcn_raw_buffer_store_b128(u32x4{pk2(o0[0], o0[1]), pk2(o0[2], o0[3]), pk2(o1[0], o1[1]), pk2(o1[2], o1[3])}, rs, lane * 16 + base + (40 + 2 * hg) * 1024, 0, 16);
    __builtin_amdgcn_raw_buffer_store_b128(u32x4{pk2(o2[0], o2[1]), pk2(o2[2], o2[3]), pk2(o3[0], o3[1]), pk2(o3[2], o3[3])}, rs, lane * 16 + base + (41 + 2 * hg) * 1024, 0, 16);
    __builtin_amdgcn_raw_buffer_store_b64(u32x2{pk2(o4[0], o4[1]), pk2(o4[2], o4[3])}, rs, lane * 8 + base + (56 + (hg >> 1)) * 1024 + (hg & 1) * 512, 0, 16);
  }
  if (w == 1) STAMP(5);
  publish(ws + WS_FLAG + (32 + rb) * 128 + hg * 8, tag, lane);
  if (w == 1) STAMP(4);
  bar();
}

__global__ __launch_bounds__(512, 2) void fused(const char* __restrict__ x, const char* __restrict__ Cr, const char* __restrict__ Ci,
                                                const char* __restrict__ Wr, const char* __restrict__ Wi, const char* __restrict__ wqkv,
                                                const char* __restrict__ wo, const char* __restrict__ gw, char* ws, char* __restrict__ out) {
  extern __shared__ __attribute__((aligned(16))) char smem_[];
  const int bid = blockIdx.x, xcd = bid & 7, ml = bid >> 6, r = (bid >> 3) & 7;
  const int w = __builtin_amdgcn_readfirstlane(threadIdx.x >> 6), lane = threadIdx.x & 63;
  const char* ws_ = ws; (void)ws_;
  if (w == 0) STAMP(0);
  if (__builtin_expect(w >= 4, 1)) {
    const int lw = w - 4;
    f32x4 gs;
    if (__builtin_expect(r < 5, 1)) gs = dft_loader(Cr, Ci, x, Wr, Wi, ws, xcd, ml, r, lw, lane);
    else gs = qkv_loader(4 * xcd + ml, r, x, wqkv, Wr, Wi, wo, ws, xcd, 3 * ml + r - 5, lw, lane);
    stage2(gs, Wr, Wi, wo, ws, out, 4 * xcd + ml, r, lw, lane);
  } else {
    if (threadIdx.x == 0) *(LDS3 u32x4*)lp(L_CNT + (u32)(size_t)(LDS3 char*)smem_) = u32x4{0u, 0u, 0u, 0u};
    const u64 tag = read_tag(ws);
    if (r < 5) dft_consumer(ws, 4 * xcd + ml, r, w, lane, tag);
    else qkv_consumer(x, gw, ws, xcd, 3 * ml + r - 5, w, lane, tag);
    if (w == 0) {
      poll(ws + WS_FLAG + (4 * xcd + ml) * 128 + (lane & 7) * 8, lane < 5, tag);
      if (lane == 0) __hip_atomic_store((LDS3 u32*)lp(L_CNT + 4), 1u, __ATOMIC_RELAXED, __HIP_MEMORY_SCOPE_WORKGROUP);
      STAMP(6);
      poll(qflag(ws, 4 * xcd + ml, lane), lane < 16, tag);
      if (lane == 0) __hip_atomic_store((LDS3 u32*)lp(L_CNT + 8), 1u, __ATOMIC_RELAXED, __HIP_MEMORY_SCOPE_WORKGROUP);
      STAMP(7);
    }
    if (bid == 0 && w == 0) {
#pragma unroll
      for (int i = 0; i < 3; i++) { const int n = i * 64 + lane; poll(ws + WS_FLAG + (n / 5) * 128 + (n % 5) * 8, n < 160, tag); }
#pragma unroll
      for (int i = 0; i < 2; i++) { const int n = i * 64 + lane; poll(ws + WS_FLAG + (32 + min(n >> 3, 11)) * 128 + (n & 7) * 8, n < 96, tag); }
      if (lane == 0) stu64(ws + WS_EPOCH, tag);
    }
  }
}

extern "C" void kernel_launch(void* const* d_in, const int* in_sizes, int n_in, void* d_out, int out_size, void* d_ws, size_t ws_size,
                              hipStream_t stream) {
  (void)hipFuncSetAttribute((const void*)fused, hipFuncAttributeMaxDynamicSharedMemorySize, LDS_TOTAL);
  fused<<<256, 512, LDS_TOTAL, stream>>>((const char*)d_in[0], (const char*)d_in[1], (const char*)d_in[2], (const char*)d_in[3],
                                         (const char*)d_in[4], (const char*)d_in[6], (const char*)d_in[7], (const char*)d_in[8],
                                         (char*)d_ws, (char*)d_out);
}
```
